```python
import torch

def _softmax(x, axis=-1):
    return torch.softmax(x, dim=axis)

def _gelu(x):
    return torch.nn.functional.gelu(x, approximate='tanh')

def _silu(x):
    return torch.nn.functional.silu(x)

def _sigmoid(x):
    return torch.sigmoid(x)

def _layernorm(x, axis=-1, eps=1e-05):
    m = x.mean(dim=axis, keepdim=True)
    v = x.var(dim=axis, keepdim=True, unbiased=False)
    return (x - m) / torch.sqrt(v + eps)

def _scatter_add(tgt, idx, src, axis=0):
    return tgt.clone().index_add_(axis, idx, src)

def _segment_sum(src, seg, n):
    out = torch.zeros((n,) + src.shape[1:], dtype=src.dtype)
    return out.index_add_(0, seg, src)

def _pad(x, pw):
    flat = tuple((v for pair in reversed(pw) for v in pair))
    return torch.nn.functional.pad(x, flat)
kernel_name = 'sequential_preLN__memory_aug_recurrent_mem_tr__mixer_s_mlp__prompt'

def setup_inputs():

    _w = lambda a: (lambda t: t.to(torch.bfloat16) if t.is_floating_point() else t)(torch.as_tensor(a))

    def _A_setup():
        B, S, Dm = (1, 2048, 640)
        H, Dh = (32, 192)
        M, seg = (8, 32)
        seg = min(seg, S)
        S = S // seg * seg
        return {'x': _w((lambda *s: torch.randn(*s, dtype=torch.bfloat16) * 0.05)(B, S, Dm)), 'mem0': _w((lambda *s: torch.randn(*s, dtype=torch.bfloat16) * 0.2)(B, M, Dm)), 'Wq': _w((lambda *s: torch.randn(*s, dtype=torch.bfloat16) * 1.6)(Dm, H, Dh) * Dm ** (-0.5)), 'Wk': _w((lambda *s: torch.randn(*s, dtype=torch.bfloat16) * 1.6)(Dm, H, Dh) * Dm ** (-0.5)), 'Wv': _w((lambda *s: torch.randn(*s, dtype=torch.bfloat16) * 0.2)(Dm, H, Dh) * Dm ** (-0.5)), 'Wo': _w((lambda *s: torch.randn(*s, dtype=torch.bfloat16) * 0.2)(H, Dh, Dm) * (H * Dh) ** (-0.5)), 'seg_len': seg}
    _A_in = _A_setup()

    def _B_setup():
        B, S, D, F = (1, 2048, 640, 1536)
        return {'x': _w((lambda *s: torch.randn(*s, dtype=torch.bfloat16) * 0.05)(B, S, D)), 'w1': _w((lambda *s: torch.randn(*s, dtype=torch.bfloat16) * 0.8)(D, D) * D ** (-0.5)), 'w2': _w((lambda *s: torch.randn(*s, dtype=torch.bfloat16) * 0.8)(D, D) * D ** (-0.5)), 'w3': _w((lambda *s: torch.randn(*s, dtype=torch.bfloat16) * 0.8)(D, F) * D ** (-0.5)), 'w4': _w((lambda *s: torch.randn(*s, dtype=torch.bfloat16) * 0.8)(F, D) * F ** (-0.5))}
    _B_in = _B_setup()
    return {'A_x': _A_in['x'], 'A_mem0': _A_in['mem0'], 'A_Wq': _A_in['Wq'], 'A_Wk': _A_in['Wk'], 'A_Wv': _A_in['Wv'], 'A_Wo': _A_in['Wo'], 'A_seg_len': _A_in['seg_len'], 'B_x': _B_in['x'], 'B_w1': _B_in['w1'], 'B_w2': _B_in['w2'], 'B_w3': _B_in['w3'], 'B_w4': _B_in['w4']}

def reference(A_x, A_mem0, A_Wq, A_Wk, A_Wv, A_Wo, A_seg_len, B_x, B_w1, B_w2, B_w3, B_w4):

    def _A_ref(x, mem0, Wq, Wk, Wv, Wo, seg_len):
        B, S, Dm = x.shape
        H, Dh = (Wq.shape[1], Wq.shape[2])
        M = mem0.shape[1]
        scale = Dh ** (-0.5)
        n_seg = S // seg_len
        L = M + seg_len
        idx = torch.arange(L)
        mask = (idx[:, None] < M) | (idx[:, None] >= idx[None, :])
        mem = mem0
        outs = []
        for s in range(n_seg):
            xs = x[:, s * seg_len:(s + 1) * seg_len, :].clone()
            h = torch.concatenate([mem, xs], axis=1)
            q = torch.einsum('btd,dhe->bhte', h, Wq)
            k = torch.einsum('btd,dhe->bhte', h, Wk)
            v = torch.einsum('btd,dhe->bhte', h, Wv)
            scores = torch.einsum('bhqe,bhke->bhqk', q, k) * scale
            scores = torch.where(mask[None, None], scores, -1000000000.0)
            p = _softmax(scores, axis=-1)
            o = torch.einsum('bhqk,bhke->bhqe', p, v)
            o = torch.einsum('bhte,hed->btd', o, Wo)
            mem = o[:, :M, :].clone()
            outs.append(o[:, M:, :])
        return torch.concatenate(outs, axis=1)

    def _B_ref(x, w1, w2, w3, w4):
        B, S, D = x.shape
        h = _gelu(_layernorm(x, axis=-1) @ w1)
        c = D // 4
        g0 = _pad(h[:, :, 0 * c:1 * c], ((0, 0), (1, 0), (0, 0)))[:, :S, :]
        g1 = _pad(h[:, :, 1 * c:2 * c], ((0, 0), (0, 1), (0, 0)))[:, 1:, :]
        g2 = _pad(h[:, :, 2 * c:3 * c], ((0, 0), (2, 0), (0, 0)))[:, :S, :]
        g3 = _pad(h[:, :, 3 * c:], ((0, 0), (0, 2), (0, 0)))[:, 2:, :]
        hs = torch.concatenate([g0, g1, g2, g3], axis=-1) @ w2
        x1 = x + hs
        h2 = _gelu(_layernorm(x1, axis=-1) @ w3) @ w4
        return x1 + h2
    x = A_x
    h = x + _A_ref(_layernorm(x, axis=-1), A_mem0, A_Wq, A_Wk, A_Wv, A_Wo, A_seg_len)
    h2 = h + _B_ref(_layernorm(h, axis=-1), B_w1, B_w2, B_w3, B_w4)
    return h2
```

```cpp
#include <hip/hip_runtime.h>
#include <hip/hip_bf16.h>

constexpr int S_ = 2048, D_ = 640, H_ = 32, E_ = 192, HE_ = H_ * E_, NSEG = 64, F_ = 1536;

#define LAS __attribute__((address_space(3)))
typedef unsigned v4u __attribute__((ext_vector_type(4)));
typedef unsigned v2u __attribute__((ext_vector_type(2)));
typedef unsigned v8u __attribute__((ext_vector_type(8)));
typedef float f4 __attribute__((ext_vector_type(4)));
typedef float f2 __attribute__((ext_vector_type(2)));
typedef __bf16 bf2v __attribute__((ext_vector_type(2)));
typedef __bf16 bf8v __attribute__((ext_vector_type(8)));
typedef __bf16 bf16v __attribute__((ext_vector_type(16)));
typedef short s4v __attribute__((ext_vector_type(4)));
typedef __amdgpu_buffer_rsrc_t rsrc_t;

constexpr unsigned MB = 1u << 20;
constexpr unsigned WS_HO = 0;
constexpr unsigned WS_PX = 4 * MB;
constexpr unsigned WS_CTRL = 9 * MB;
constexpr unsigned WS_FILL_END = 9 * MB + 128 * 1024;
constexpr unsigned WS_XF = 10 * MB;
constexpr unsigned WS_HF = 13 * MB;
constexpr unsigned WS_H1F = 16 * MB;
constexpr unsigned WS_X1F = 19 * MB;
constexpr unsigned WS_H2F = 22 * MB;
constexpr unsigned WS_WF = 29 * MB;
constexpr unsigned WS_CS = 35 * MB;
constexpr unsigned WS_BF = 36 * MB;
constexpr unsigned WS_QK = 62 * MB;
constexpr unsigned WS_UX = 111 * MB;
constexpr unsigned WS_END = 192 * MB;

constexpr unsigned HO_P = 0, HO_MEM = 20480;
constexpr unsigned C_TICKET = 0, C_POL = 8, C_TMO = 9, C_BCNT = 16, C_WFLAG = 32;
constexpr unsigned SENT = 0xFFFFFFFFu;
constexpr unsigned SPINCAP = 1u << 18;
constexpr int LDS_BYTES = 160 * 1024;

constexpr unsigned WF1 = 0, WF2 = WF1 + 20 * 20 * 2048, WF3 = WF2 + 20 * 20 * 2048, WF4 = WF3 + 48 * 20 * 2048;
constexpr unsigned CS1 = 0, CS3 = 640 * 4;

struct Params {
  const unsigned short* x; const unsigned short* mem0;
  const unsigned short* wq; const unsigned short* wk; const unsigned short* wv; const unsigned short* wo;
  const unsigned short* w1; const unsigned short* w2; const unsigned short* w3; const unsigned short* w4;
  unsigned short* out; unsigned char* ws;
};

__device__ __forceinline__ unsigned char* ws_base(unsigned char* ws) {
  return ws + ((0u - (unsigned)(size_t)ws) & (2 * MB - 1));
}
__device__ __forceinline__ rsrc_t mk_rsrc(const void* p, unsigned bytes) {
  return __builtin_amdgcn_make_buffer_rsrc((void*)p, 0, bytes, 0x00020000);
}
__device__ __forceinline__ v4u ld16c(rsrc_t r, unsigned v, unsigned s) { return __builtin_amdgcn_raw_buffer_load_b128(r, v, s, 16); }
__device__ __forceinline__ unsigned ld4c(rsrc_t r, unsigned v, unsigned s) { return __builtin_amdgcn_raw_buffer_load_b32(r, v, s, 16); }
__device__ __forceinline__ void st16c(rsrc_t r, unsigned v, unsigned s, v4u d) { __builtin_amdgcn_raw_buffer_store_b128(d, r, v, s, 16); }
__device__ __forceinline__ void st8c(rsrc_t r, unsigned v, unsigned s, v2u d) { __builtin_amdgcn_raw_buffer_store_b64(d, r, v, s, 16); }
__device__ __forceinline__ void st4c(rsrc_t r, unsigned v, unsigned s, unsigned d) { __builtin_amdgcn_raw_buffer_store_b32(d, r, v, s, 16); }
__device__ __forceinline__ void st2c(rsrc_t r, unsigned v, unsigned s, unsigned short d) { __builtin_amdgcn_raw_buffer_store_b16(d, r, v, s, 16); }
__device__ __forceinline__ v4u ld16p(rsrc_t r, unsigned v, unsigned s) { return __builtin_amdgcn_raw_buffer_load_b128(r, v, s, 0); }
__device__ __forceinline__ unsigned ld4p(rsrc_t r, unsigned v, unsigned s) { return __builtin_amdgcn_raw_buffer_load_b32(r, v, s, 0); }

__device__ __forceinline__ unsigned f2u(float a) { return __builtin_bit_cast(unsigned, a); }
__device__ __forceinline__ float u2f(unsigned a) { return __builtin_bit_cast(float, a); }
__device__ __forceinline__ unsigned pk2(float a, float b) { bf2v t = {(__bf16)a, (__bf16)b}; return __builtin_bit_cast(unsigned, t); }
__device__ __forceinline__ float bflo(unsigned u) { return __builtin_bit_cast(float, u << 16); }
__device__ __forceinline__ float bfhi(unsigned u) { return __builtin_bit_cast(float, u & 0xFFFF0000u); }
__device__ __forceinline__ float rbf(float a) { return (float)(__bf16)a; }
__device__ __forceinline__ v4u pk8(f4 a, f4 b) { return (v4u){pk2(a[0], a[1]), pk2(a[2], a[3]), pk2(b[0], b[1]), pk2(b[2], b[3])}; }

__device__ __forceinline__ f4 mfma32(v4u a, v4u b, f4 c) {
  return __builtin_amdgcn_mfma_f32_16x16x32_bf16(__builtin_bit_cast(bf8v, a), __builtin_bit_cast(bf8v, b), c, 0, 0, 0);
}
__device__ __forceinline__ f4 smfmac64(v4u a, v8u b, f4 c, int idx) {
  return __builtin_amdgcn_smfmac_f32_16x16x64_bf16(__builtin_bit_cast(bf8v, a), __builtin_bit_cast(bf16v, b), c, idx, 0, 0);
}
__device__ __forceinline__ v2u trread(unsigned lds_addr) {
  return __builtin_bit_cast(v2u, __builtin_amdgcn_ds_read_tr16_b64_v4i16((LAS s4v*)lds_addr));
}
__device__ __forceinline__ v4u lds16(unsigned a) { return *(LAS v4u*)a; }
__device__ __forceinline__ void lds16w(unsigned a, v4u v) { *(LAS v4u*)a = v; }
__device__ __forceinline__ void lds4w(unsigned a, unsigned v) { *(LAS unsigned*)a = v; }
__device__ __forceinline__ unsigned lds4(unsigned a) { return *(LAS unsigned*)a; }

__device__ __forceinline__ void glds16(const void* gsrc, unsigned dst) {
  unsigned keep;
  asm volatile("s_mov_b32 %0, m0\n\ts_mov_b32 m0, %2\n\ts_nop 0\n\tglobal_load_lds_dwordx4 %1, off\n\ts_mov_b32 m0, %0"
               : "=&s"(keep) : "v"(gsrc), "s"(dst) : "memory");
}
#define VMWAIT(n) asm volatile("s_waitcnt vmcnt(" #n ")" ::: "memory")
__device__ __forceinline__ void bar() {
  asm volatile("s_waitcnt lgkmcnt(0)" ::: "memory");
  __builtin_amdgcn_s_barrier();
  asm volatile("" ::: "memory");
}
__device__ __forceinline__ unsigned mx4(v4u v) { return max(max(v.x, v.y), max(v.z, v.w)); }
__device__ __forceinline__ unsigned or4(v4u v) { return (v.x | v.y) | (v.z | v.w); }
__device__ __forceinline__ unsigned ho_phys(unsigned o) { return ((o >> 13) << 14) | (o & 8191u); }

__device__ __forceinline__ float xor16_max(float v) { auto r = __builtin_amdgcn_permlane16_swap(f2u(v), f2u(v), false, false);
  return fmaxf(u2f(r[0]), u2f(r[1])); }
__device__ __forceinline__ float xor32_max(float v) { auto r = __builtin_amdgcn_permlane32_swap(f2u(v), f2u(v), false, false);
  return fmaxf(u2f(r[0]), u2f(r[1])); }
__device__ __forceinline__ float xor16_add(float v) { auto r = __builtin_amdgcn_permlane16_swap(f2u(v), f2u(v), false, false);
  return u2f(r[0]) + u2f(r[1]); }
__device__ __forceinline__ float xor32_add(float v) { auto r = __builtin_amdgcn_permlane32_swap(f2u(v), f2u(v), false, false);
  return u2f(r[0]) + u2f(r[1]); }
__device__ __forceinline__ float ror8(float v) {
  return __builtin_bit_cast(float, __builtin_amdgcn_update_dpp(0, __builtin_bit_cast(int, v), 0x128, 0xf, 0xf, true));
}
__device__ __forceinline__ unsigned fold_pack(f4 a) {
  auto r0 = __builtin_amdgcn_permlane32_swap(f2u(a[0]), f2u(a[2]), false, false);
  auto r1 = __builtin_amdgcn_permlane32_swap(f2u(a[1]), f2u(a[3]), false, false);
  return pk2(u2f(r0[0]) + u2f(r0[1]), u2f(r1[0]) + u2f(r1[1]));
}
__device__ __forceinline__ int wait_ge(rsrc_t rw, unsigned word, int want, bool& dead) {
  int v = -1;
  for (unsigned sp = 0; !dead; ++sp) {
    asm volatile("" ::: "memory");
    v = (int)__builtin_amdgcn_readfirstlane(ld4c(rw, 0, WS_CTRL + word * 256));
    if (v >= want) break;
    if (sp > SPINCAP) { dead = true; st4c(rw, 0, WS_CTRL + C_TMO * 256, word); }
    __builtin_amdgcn_s_sleep(2);
  }
  __builtin_amdgcn_fence(__ATOMIC_ACQUIRE, "wavefront");
  return v;
}

constexpr int INIT_LN = 128, INIT_WT = 108 * 4, INIT_FILL = 592;
__global__ void __launch_bounds__(256) k_init(Params p) {
  __shared__ float red[2][4][16];
  unsigned char* ws = ws_base(p.ws);
  const int tid = threadIdx.x, lane = tid & 63, w = tid >> 6, g = lane >> 4, n = lane & 15;
  int b = blockIdx.x;
  if (b < INIT_LN) {
    const int s = b >> 1, rt = b & 1;
    const int tok = s * 32 + 8 * (n >> 2) + 4 * rt + (n & 3);
    v4u v[5]; float sum = 0.f;
#pragma unroll
    for (int k = 0; k < 5; ++k) v[k] = *(const v4u*)(p.x + (size_t)tok * D_ + (w * 5 + k) * 32 + g * 8);
#pragma unroll
    for (int k = 0; k < 5; ++k)
#pragma unroll
      for (int j = 0; j < 4; ++j) sum += bflo(v[k][j]) + bfhi(v[k][j]);
    sum = xor32_add(xor16_add(sum));
    if (g == 0) red[0][w][n] = sum;
    __syncthreads();
    const float mu = (red[0][0][n] + red[0][1][n] + red[0][2][n] + red[0][3][n]) * (1.f / D_);
    float sq = 0.f;
#pragma unroll
    for (int k = 0; k < 5; ++k)
#pragma unroll
      for (int j = 0; j < 4; ++j) { float a = bflo(v[k][j]) - mu, c = bfhi(v[k][j]) - mu; sq += a * a + c * c; }
    sq = xor32_add(xor16_add(sq));
    if (g == 0) red[1][w][n] = sq;
    __syncthreads();
    const float var = (red[1][0][n] + red[1][1][n] + red[1][2][n] + red[1][3][n]) * (1.f / D_);
    const float mub = rbf(mu), sd = rbf(sqrtf(rbf(rbf(var) + 1e-5f)));
#pragma unroll
    for (int k = 0; k < 5; ++k) {
      v4u o;
#pragma unroll
      for (int j = 0; j < 4; ++j) o[j] = pk2(rbf(bflo(v[k][j]) - mub) / sd, rbf(bfhi(v[k][j]) - mub) / sd);
      *(v4u*)(ws + WS_XF + ((size_t)(s * 20 + w * 5 + k) * 2 + rt) * 1024 + lane * 16) = o;
    }
    return;
  }
  b -= INIT_LN;
  if (b < INIT_WT) {
    const int slab = b >> 2, kq = b & 3;
    const unsigned short* W; int N, KS, j; unsigned dst;
    if (slab < 20) { W = p.w1; N = D_; KS = 20; j = slab; dst = WF1; }
    else if (slab < 40) { W = p.w2; N = D_; KS = 20; j = slab - 20; dst = WF2; }
    else if (slab < 88) { W = p.w3; N = F_; KS = 20; j = slab - 40; dst = WF3; }
    else { W = p.w4; N = D_; KS = 48; j = slab - 88; dst = WF4; }
    const int per = KS / 4;
    for (int u = w; u < per * 2; u += 4) {
      const int ks = kq * per + (u >> 1), tt = u & 1;
      const int col = j * 32 + 8 * (n >> 2) + 4 * tt + (n & 3);
      unsigned short e[8];
#pragma unroll
      for (int q = 0; q < 8; ++q) e[q] = W[(size_t)(ks * 32 + g * 8 + q) * N + col];
      v4u o = {e[0] | ((unsigned)e[1] << 16), e[2] | ((unsigned)e[3] << 16), e[4] | ((unsigned)e[5] << 16), e[6] | ((unsigned)e[7] << 16)};
      *(v4u*)(ws + WS_WF + dst + ((size_t)(j * KS + ks) * 2 + tt) * 1024 + lane * 16) = o;
    }
    if (kq == 0 && (slab < 20 || (slab >= 40 && slab < 88))) {
      const int c = tid & 31, part = tid >> 5;
      float s = 0.f;
      for (int k = part; k < D_; k += 8) s += bflo(W[(size_t)k * N + j * 32 + c]);
      __shared__ float cs[8][32];
      cs[part][c] = s;
      __syncthreads();
      if (tid < 32) { float t = 0.f; for (int q = 0; q < 8; ++q) t += cs[q][tid];
        *(float*)(ws + WS_CS + (slab < 20 ? CS1 : CS3) + (j * 32 + tid) * 4) = t; }
    }
    return;
  }
  b -= INIT_WT;
#pragma unroll
  for (int k = 0; k < 4; ++k) {
    const unsigned o = (unsigned)b * 16384u + k * 4096u + tid * 16u;
    if (o >= WS_FILL_END) break;
    v4u v = {SENT, SENT, SENT, SENT};
    if (o < 65536u) {
      const unsigned lo = ((o >> 14) << 13) | (o & 8191u);
      if (lo >= HO_MEM && lo < HO_MEM + 10240u) {
        const unsigned c = (lo - HO_MEM) >> 7, i = (lo >> 4) & 7;
        const unsigned* m32 = (const unsigned*)p.mem0;
#pragma unroll
        for (int q = 0; q < 4; ++q) { unsigned t = m32[i * 320 + 8 * (c >> 1) + 2 * q + (c & 1)]; v[q] = (t == SENT) ? 0x7FC07FC0u : t; }
      }
    }
    *(v4u*)(ws + o) = v;
  }
}

__device__ __forceinline__ void bgemm_unit(const Params& p, rsrc_t rw, int h, int part, int lane, int w) {
  const int g = lane >> 4, n = lane & 15;
#pragma unroll
  for (int i = 0; i < 12; ++i) {
    const int pos = (w * 12 + i) * 64 + lane, e = pos >> 4, c16 = pos & 15;
    const int f = (e & 3) | (((e >> 3) & 1) << 2);
    glds16(p.wo + ((size_t)(h * E_ + e) * D_ + part * 128 + (((c16 >> 1) ^ f) * 16) + (c16 & 1) * 8), (w * 12 + i) * 1024);
  }
  rsrc_t rv = mk_rsrc(p.wv, D_ * HE_ * 2);
  const unsigned aoff = ((unsigned)(w * 160 + 8 * (n >> 2) + (n & 3)) * HE_ + h * E_ + g * 8) * 2;
  v4u a[2][12];
#pragma unroll
  for (int k = 0; k < 12; ++k) a[0][k] = ld16p(rv, aoff, ((k & 1) * 4 * HE_ + (k >> 1) * 32) * 2);
  VMWAIT(12);
  bar();
  v4u bw[6][8];
  {
    const int r = (lane >> 2) & 3, cc = lane & 3, f = r | ((g & 1) << 2);
#pragma unroll
    for (int nt = 0; nt < 8; ++nt) {
      const unsigned base = (8 * g + r) * 256 + ((nt ^ f) * 32) + cc * 8;
#pragma unroll
      for (int ks = 0; ks < 6; ++ks) { v2u lo = trread(base + ks * 8192), hi = trread(base + ks * 8192 + 1024); bw[ks][nt] = (v4u){lo.x, lo.y, hi.x, hi.y}; }
    }
  }
  const unsigned doff = (unsigned)(((n >> 1) & 1) * 16 * 20 * 1024 + (g * 16 + (h & 1) * 8 + 2 * (n >> 2) + (n & 1)) * 16);
#pragma unroll
  for (int i = 0; i < 5; ++i) {
    if (i + 1 < 5) {
#pragma unroll
      for (int k = 0; k < 12; ++k) a[(i + 1) & 1][k] = ld16p(rv, aoff, (((i + 1) * 32 + (k & 1) * 4) * HE_ + (k >> 1) * 32) * 2);
    }
    f4 acc[2][8];
#pragma unroll
    for (int nt = 0; nt < 8; ++nt) { acc[0][nt] = (f4){0, 0, 0, 0}; acc[1][nt] = (f4){0, 0, 0, 0}; }
#pragma unroll
    for (int ks = 0; ks < 6; ++ks)
#pragma unroll
      for (int nt = 0; nt < 8; ++nt) { acc[0][nt] = mfma32(a[i & 1][2 * ks], bw[ks][nt], acc[0][nt]); acc[1][nt] = mfma32(a[i & 1][2 * ks + 1], bw[ks][nt], acc[1][nt]); }
    const int dp = w * 5 + i;
#pragma unroll
    for (int nt = 0; nt < 8; ++nt)
      st16c(rw, doff, WS_BF + (unsigned)(((2 * (8 * part + nt)) * 16 + (h >> 1)) * 20 + dp) * 1024u, pk8(acc[0][nt], acc[1][nt]));
  }
  VMWAIT(0);
  bar();
  if (w == 0 && lane == 0) __hip_atomic_fetch_add((unsigned*)(ws_base(p.ws) + WS_CTRL + (C_BCNT + part) * 256), 1u, __ATOMIC_RELAXED, __HIP_MEMORY_SCOPE_AGENT);
}

template <bool QK>
__device__ __forceinline__ void w_loop(unsigned char* ws, rsrc_t rw, const v4u (&wt)[4][20], unsigned outbase, unsigned outstep, int flagword, int lane, int w) {
  const unsigned char* xsrc = ws + WS_XF + w * 10240 + lane * 16;
#pragma unroll
  for (int i = 0; i < 10; ++i) glds16(xsrc + i * 1024, w * 10240 + i * 1024);
  VMWAIT(0);
  for (int s = 0; s < NSEG; ++s) {
    if (s == 1 || s == 2) VMWAIT(0);
    bar();
    if (w == 0 && lane == 0 && s >= 1) st4c(rw, 0, WS_CTRL + flagword * 256, (unsigned)(s <= 2 ? s - 1 : s - 2));
    const unsigned cur = (s & 1) * 40960u, nxt = 40960u - cur;
    if (s + 1 < NSEG) {
#pragma unroll
      for (int i = 0; i < 10; ++i) glds16(xsrc + (size_t)(s + 1) * 40960 + i * 1024, nxt + w * 10240 + i * 1024);
    }
    f4 acc[4][2];
#pragma unroll
    for (int t = 0; t < 4; ++t) { acc[t][0] = (f4){0, 0, 0, 0}; acc[t][1] = (f4){0, 0, 0, 0}; }
#pragma unroll
    for (int ks = 0; ks < 20; ++ks) {
      const v4u x0 = lds16(cur + ks * 2048 + lane * 16), x1 = lds16(cur + ks * 2048 + 1024 + lane * 16);
#pragma unroll
      for (int t = 0; t < 4; ++t) {
        if (QK) { acc[t][0] = mfma32(wt[t][ks], x0, acc[t][0]); acc[t][1] = mfma32(wt[t][ks], x1, acc[t][1]); }
        else { acc[t][0] = mfma32(x0, wt[t][ks], acc[t][0]); acc[t][1] = mfma32(x1, wt[t][ks], acc[t][1]); }
      }
    }
    VMWAIT(0);
    const unsigned ob = outbase + (unsigned)s * outstep;
    if (QK) {
#pragma unroll
      for (int q = 0; q < 4; ++q) st16c(rw, lane * 16, ob + q * 1024, pk8(acc[2 * (q >> 1)][q & 1], acc[2 * (q >> 1) + 1][q & 1]));
    } else {
#pragma unroll
      for (int t = 0; t < 4; ++t) st16c(rw, lane * 16, ob + t * 1024, pk8(acc[t][0], acc[t][1]));
    }
  }
  VMWAIT(0);
  bar();
  if (w == 0 && lane == 0) st4c(rw, 0, WS_CTRL + flagword * 256, (unsigned)(NSEG - 1));
}

__device__ __forceinline__ void role_wu(const Params& p, unsigned char* ws, rsrc_t rw, int c, int lane, int w) {
  bgemm_unit(p, rw, 16 + 4 * (c & 3) + (c >> 2) / 5, (c >> 2) % 5, lane, w);
  bool dead = false;
  wait_ge(rw, C_BCNT + (c >> 4), 31, dead);
  v4u wt[4][20];
#pragma unroll
  for (int t = 0; t < 4; ++t)
#pragma unroll
    for (int k = 0; k < 20; ++k) wt[t][k] = ld16c(rw, lane * 16, WS_BF + (unsigned)((c * 16 + w * 4 + t) * 20 + k) * 1024u);
  w_loop<false>(ws, rw, wt, WS_UX + (unsigned)(c * 16 + w * 4) * 1024u, 80u * 16384u, C_WFLAG + c, lane, w);
}

__device__ __forceinline__ void role_wqk(const Params& p, unsigned char* ws, rsrc_t rw, int j, int lane, int w) {
  const int wu = 4 * j + w, h = wu / 6, qk = (wu % 6) / 3, e3 = wu % 3;
  const int g = lane >> 4, r = (lane >> 2) & 3, cc = lane & 3;
  const int dr = lane >> 3, c16 = lane & 7, fs = ((dr >> 1) & 1);
  const unsigned short* W = (qk ? p.wk : p.wq) + h * E_ + e3 * 64;
  const unsigned lb = w * 40960;
  auto issue = [&](int blk) {
#pragma unroll
    for (int i = 0; i < 8; ++i) {
      const int f = fs | ((i & 1) << 1);
      glds16(W + (size_t)(blk * 64 + i * 8 + dr) * HE_ + (((c16 >> 1) ^ f) * 16) + (c16 & 1) * 8, lb + (blk % 5) * 8192 + i * 1024);
    }
  };
#pragma unroll
  for (int b = 0; b < 5; ++b) issue(b);
  v4u wt[4][20];
  const int f = (r >> 1) | ((g & 1) << 1);
#pragma unroll
  for (int b = 0; b < 10; ++b) {
    if (b <= 5) VMWAIT(32); else if (b == 6) VMWAIT(24); else if (b == 7) VMWAIT(16); else if (b == 8) VMWAIT(8); else VMWAIT(0);
#pragma unroll
    for (int t = 0; t < 4; ++t) {
      const unsigned base = lb + (b % 5) * 8192 + (8 * g + r) * 128 + ((t ^ f) * 32) + cc * 8;
#pragma unroll
      for (int hf = 0; hf < 2; ++hf) { v2u lo = trread(base + hf * 4096), hi = trread(base + hf * 4096 + 512); wt[t][2 * b + hf] = (v4u){lo.x, lo.y, hi.x, hi.y}; }
    }
    asm volatile("s_waitcnt lgkmcnt(0)" ::: "memory");
    if (b + 5 < 10) issue(b + 5);
  }
  bar();
  w_loop<true>(ws, rw, wt, WS_QK + (unsigned)(h * 24 + qk * 12 + e3 * 4) * 1024u, 32u * 24576u, C_WFLAG + 80 + j, lane, w);
}

constexpr unsigned Y_IMG = 122880, Y_KX = Y_IMG + 12288;
__device__ __forceinline__ constexpr int y_blk_qk(int i) { return i < 15 ? (i < 8 ? 0 : 1) : ((i - 15) & 1 ? 0 : 1); }
__device__ __forceinline__ constexpr int y_blk_ks(int i) {
  constexpr int qs[8] = {0, 1, 3, 4, 5, 7, 8, 9};
  constexpr int kk[7] = {1, 2, 3, 5, 6, 7, 9};
  return i < 8 ? qs[i] : (i < 15 ? kk[i - 8] : 2 * (i - 15));
}
__device__ __forceinline__ constexpr int y_find(int qk, int ks) { for (int i = 0; i < 20; ++i) if (y_blk_qk(i) == qk && y_blk_ks(i) == ks) return i; return -1; }

__device__ __forceinline__ void role_y(const Params& p, unsigned char* ws, rsrc_t rw, int h, int lane, int w) {
  const int g = lane >> 4, n = lane & 15, r = (lane >> 2) & 3, cc = lane & 3;
  bool dead = false;
  unsigned soff[6];
#pragma unroll
  for (int i = 0; i < 6; ++i) {
    const int pos = (w * 6 + i) * 64 + lane, d = pos / 24, c16 = pos % 24;
    const int f = ((d >> 1) & 1) | (((d >> 3) & 1) << 1);
    soff[i] = (unsigned)(d * HE_ + h * E_ + (((c16 >> 1) ^ f) * 16) + (c16 & 1) * 8);
  }
  auto issue = [&](int i) {
    const unsigned short* W = (y_blk_qk(i) ? p.wk : p.wq) + (size_t)y_blk_ks(i) * 64 * HE_;
#pragma unroll
    for (int q = 0; q < 6; ++q) glds16(W + soff[q], (i % 5) * 24576 + (w * 6 + q) * 1024);
  };
  unsigned tb[3];
  { const int f = (r >> 1) | ((g & 1) << 1);
#pragma unroll
    for (int t = 0; t < 3; ++t) tb[t] = (8 * g + r) * 384 + (((3 * w + t) ^ f) * 32) + cc * 8; }
  auto rdfrag = [&](int slot, int t) -> v8u {
    const unsigned a = tb[t] + slot * 24576;
    v2u q0 = trread(a), q1 = trread(a + 4 * 384), q2 = trread(a + 32 * 384), q3 = trread(a + 36 * 384);
    return (v8u){q0.x, q0.y, q1.x, q1.y, q2.x, q2.y, q3.x, q3.y};
  };
#pragma unroll
  for (int i = 0; i < 5; ++i) issue(i);
  v8u wt[15][3];
#pragma unroll
  for (int i = 0; i < 15; ++i) {
    VMWAIT(24);
    bar();
#pragma unroll
    for (int t = 0; t < 3; ++t) wt[i][t] = rdfrag(i % 5, t);
    bar();
    issue(i + 5);
  }
  VMWAIT(0);
  const int pol = wait_ge(rw, C_POL, 0, dead);
  const int jf0 = C_WFLAG + 80 + (6 * h) / 4;
  int seen = -1;
  auto need_qk = [&](int s) { if (seen < s) { int a = wait_ge(rw, jf0, s, dead), b = wait_ge(rw, jf0 + 1, s, dead); seen = min(a, b); } };
  auto load_kx = [&](int s) {
    need_qk(s);
    v4u t[12];
#pragma unroll
    for (int i = 0; i < 12; ++i) t[i] = ld16c(rw, lane * 16, WS_QK + (unsigned)((s * 32 + h) * 24 + 12 + i) * 1024u);
#pragma unroll
    for (int i = 0; i < 12; ++i) lds16w(Y_KX + (s & 1) * 12288 + i * 1024 + lane * 16, t[i]);
  };
  if (w == 3) load_kx(0);
  bar();
  const int sidx = (lane & 8) ? 0xEEEE : 0x4444;
  const float cexp = 0.07216878364870322f * 1.4426950408889634f;
  const unsigned imgw = n * 16 + (g & 1) * 8 + (g >> 1) * 4;
  const unsigned imgr = (4 * g + r) * 16 + (cc & 1) * 8;
  for (int s = 0; s < NSEG; ++s) {
    const unsigned hb = WS_HO + (unsigned)s * 65536u + (((pol >> (s >> 5)) & 1) << 13);
    v4u a[10];
    for (unsigned sp = 0;; ++sp) {
      asm volatile("" ::: "memory");
      unsigned m = 0;
#pragma unroll
      for (int k = 0; k < 10; ++k) a[k] = ld16c(rw, lane * 16, hb + ho_phys(HO_MEM + k * 1024));
#pragma unroll
      for (int k = 0; k < 10; ++k) m = max(m, mx4(a[k]));
      if (__all(m != SENT) || dead) break;
      if (sp > SPINCAP) { dead = true; st4c(rw, 0, WS_CTRL + C_TMO * 256, 1000 + s); }
    }
    f4 acc[6];
#pragma unroll
    for (int t = 0; t < 6; ++t) acc[t] = (f4){0, 0, 0, 0};
#pragma unroll
    for (int ks = 0; ks < 10; ++ks)
#pragma unroll
      for (int t = 0; t < 6; ++t) {
        const int bi = y_find(t / 3, ks);
        if (bi < 15) acc[t] = smfmac64(a[ks], wt[bi < 15 ? bi : 0][t % 3], acc[t], sidx);
        else acc[t] = smfmac64(a[ks], rdfrag(bi % 5, t % 3), acc[t], sidx);
      }
    const unsigned img = Y_IMG + (s & 1) * 6144;
#pragma unroll
    for (int t = 0; t < 6; ++t) lds4w(img + ((t / 3) * 12 + 3 * w + t % 3) * 256 + imgw, fold_pack(acc[t]));
    bar();
    const unsigned kxb = Y_KX + (s & 1) * 12288 + lane * 16;
    if (w == 0) {
      if (s + 1 < NSEG) {
        f4 d0 = {0, 0, 0, 0}, d1 = d0, d2 = d0;
#pragma unroll
        for (int u = 0; u < 6; ++u) {
          v2u q0 = trread(img + (2 * u) * 256 + imgr), q1 = trread(img + (2 * u + 1) * 256 + imgr);
          v2u k0 = trread(img + (12 + 2 * u) * 256 + imgr), k1 = trread(img + (13 + 2 * u) * 256 + imgr);
          const v4u qf = {q0.x, q0.y, q1.x, q1.y}, kf = {k0.x, k0.y, k1.x, k1.y};
          d1 = mfma32(lds16(kxb + u * 2048), qf, d1);
          d2 = mfma32(lds16(kxb + u * 2048 + 1024), qf, d2);
          d0 = mfma32(kf, qf, d0);
        }
        const bool hi = (n & 8) != 0;
        float v[5];
#pragma unroll
        for (int i = 0; i < 4; ++i) v[i] = hi ? d2[i] : d1[i];
        { const float e0 = hi ? d0[1] : d0[0], e1 = hi ? d0[3] : d0[2]; v[4] = (g & 2) ? e1 : e0; }
        float m = fmaxf(fmaxf(fmaxf(v[0], v[1]), fmaxf(v[2], v[3])), v[4]);
        m = fmaxf(m, ror8(m)); m = xor32_max(xor16_max(m));
        const float mc = m * cexp;
        float sum = 0.f;
#pragma unroll
        for (int i = 0; i < 5; ++i) { v[i] = __builtin_amdgcn_exp2f(v[i] * cexp - mc); sum += v[i]; }
        sum += ror8(sum); sum = xor32_add(xor16_add(sum));
        const float rs = __builtin_amdgcn_rcpf(sum);
        const v2u px = {pk2(v[0] * rs, v[1] * rs) & 0x7FFF7FFFu, pk2(v[2] * rs, v[3] * rs) & 0x7FFF7FFFu};
        const unsigned pm = pk2(v[4] * rs, 0.f) & 0x7FFFu;
        st8c(rw, (2 * g) * 128 + (n & 7) * 16 + (n >> 3) * 8, hb + ho_phys(HO_P + (32 + 8 * (h >> 1) + (h & 1)) * 128), px);
        st2c(rw, (n & 7) * 16 + (4 * (g & 1) + 2 * (g >> 1) + (n >> 3)) * 2, hb + ho_phys(HO_P + h * 128), (unsigned short)pm);
      }
    } else if (w == 3) {
      if (s + 1 < NSEG) load_kx(s + 1);
    } else {
      const int rq = w - 1, tok = 16 * rq + n;
      need_qk(s);
      const unsigned qo = ((tok >> 2) & 1) * 1024 + (g * 16 + 4 * (tok >> 3) + (tok & 3)) * 16;
      v4u qf[6];
#pragma unroll
      for (int u = 0; u < 6; ++u) qf[u] = ld16c(rw, qo, WS_QK + (unsigned)((s * 32 + h) * 24 + 2 * u) * 1024u);
      f4 d0 = {0, 0, 0, 0}, d1 = d0, d2 = d0;
#pragma unroll
      for (int u = 0; u < 6; ++u) {
        v2u k0 = trread(img + (12 + 2 * u) * 256 + imgr), k1 = trread(img + (13 + 2 * u) * 256 + imgr);
        const v4u kf = {k0.x, k0.y, k1.x, k1.y};
        d1 = mfma32(lds16(kxb + u * 2048), qf[u], d1);
        d2 = mfma32(lds16(kxb + u * 2048 + 1024), qf[u], d2);
        d0 = mfma32(kf, qf[u], d0);
      }
      float v[10];
      const float NEG = -1.0e9f * 1.4426950408889634f;
#pragma unroll
      for (int i = 0; i < 4; ++i) { v[i] = (8 * g + i <= tok) ? d1[i] * cexp : NEG; v[4 + i] = (8 * g + 4 + i <= tok) ? d2[i] * cexp : NEG; }
      v[8] = ((g & 2) ? d0[2] : d0[0]) * cexp; v[9] = ((g & 2) ? d0[3] : d0[1]) * cexp;
      float m = v[0];
#pragma unroll
      for (int i = 1; i < 10; ++i) m = fmaxf(m, v[i]);
      m = xor32_max(xor16_max(m));
      float sum = 0.f;
#pragma unroll
      for (int i = 0; i < 10; ++i) { v[i] = __builtin_amdgcn_exp2f(v[i] - m); sum += v[i]; }
      sum = xor32_add(xor16_add(sum));
      const float rs = __builtin_amdgcn_rcpf(sum);
      v4u px;
#pragma unroll
      for (int i = 0; i < 4; ++i) px[i] = pk2(v[2 * i] * rs, v[2 * i + 1] * rs) & 0x7FFF7FFFu;
      const unsigned pb = WS_PX + (unsigned)s * 81920u;
      st16c(rw, lane * 16, pb + (h * 2 + rq) * 1024, px);
      st4c(rw, (((h >> 1) & 3) * 16 + n) * 16 + (2 * (g & 1) + (g >> 1)) * 4, pb + 65536 + (((h >> 3) * 2 + (h & 1)) * 2 + rq) * 1024,
           pk2(v[8] * rs, v[9] * rs) & 0x7FFF7FFFu);
    }
  }
}

constexpr unsigned Z_W = 0, Z_U = 98304, Z_M = Z_U + 40960, Z_R = Z_M + 10240;
__device__ __forceinline__ void role_z(const Params& p, unsigned char* ws, rsrc_t rw, int c, int lane, int w) {
  bgemm_unit(p, rw, 4 * (c & 3) + (c >> 2) / 5, (c >> 2) % 5, lane, w);
  const int g = lane >> 4, n = lane & 15;
  bool dead = false;
  wait_ge(rw, C_BCNT + (c >> 4), 31, dead);
  v8u wt[4][7];
#pragma unroll
  for (int t = 0; t < 4; ++t) {
    const unsigned b = WS_BF + (unsigned)((c * 16 + w * 4 + t) * 20) * 1024u;
#pragma unroll
    for (int k = 0; k < 7; ++k) { v4u lo = ld16c(rw, lane * 16, b + (2 * k) * 1024), hi = ld16c(rw, lane * 16, b + (2 * k + 1) * 1024);
      wt[t][k] = (v8u){lo.x, lo.y, lo.z, lo.w, hi.x, hi.y, hi.z, hi.w}; }
#pragma unroll
    for (int k = 14; k < 20; ++k) lds16w(Z_W + ((w * 4 + t) * 6 + (k - 14)) * 1024 + lane * 16, ld16c(rw, lane * 16, b + k * 1024));
  }
  const int pol = wait_ge(rw, C_POL, 0, dead);
  const int sidx = (lane & 8) ? 0xEEEE : 0x4444;
  int seen = -1;
  rsrc_t rx = mk_rsrc(p.x, S_ * D_ * 2);
  for (int s = 0; s < NSEG; ++s) {
    const unsigned hb = WS_HO + (unsigned)s * 65536u + (((pol >> (s >> 5)) & 1) << 13);
    const unsigned ub = Z_U + (s & 1) * 20480;
    if (seen < s) seen = wait_ge(rw, C_WFLAG + c, s, dead);
    v4u ux[4];
#pragma unroll
    for (int t = 0; t < 4; ++t) ux[t] = ld16c(rw, lane * 16, WS_UX + (unsigned)((s * 80 + c) * 16 + w * 4 + t) * 1024u);
    {
      const int f0 = w, f1 = w + 4, f2 = w < 2 ? w + 8 : w;
      v4u a0, a1, a2;
      for (unsigned sp = 0;; ++sp) {
        asm volatile("" ::: "memory");
        a0 = ld16c(rw, lane * 16, hb + ho_phys(HO_MEM + f0 * 1024)); a1 = ld16c(rw, lane * 16, hb + ho_phys(HO_MEM + f1 * 1024)); a2 = ld16c(rw, lane * 16, hb + ho_phys(HO_MEM + f2 * 1024));
        if (__all(max(max(mx4(a0), mx4(a1)), mx4(a2)) != SENT) || dead) break;
        if (sp > SPINCAP) { dead = true; st4c(rw, 0, WS_CTRL + C_TMO * 256, 2000 + s); }
      }
      lds16w(Z_M + f0 * 1024 + lane * 16, a0); lds16w(Z_M + f1 * 1024 + lane * 16, a1); lds16w(Z_M + f2 * 1024 + lane * 16, a2);
    }
#pragma unroll
    for (int t = 0; t < 4; ++t) lds16w(ub + (4 + w * 4 + t) * 1024 + lane * 16, ux[t]);
    bar();
    {
      f4 acc[4];
#pragma unroll
      for (int t = 0; t < 4; ++t) acc[t] = (f4){0, 0, 0, 0};
#pragma unroll
      for (int ks = 0; ks < 10; ++ks) {
        const v4u a = lds16(Z_M + ks * 1024 + lane * 16);
#pragma unroll
        for (int t = 0; t < 4; ++t) {
          if (ks < 7) acc[t] = smfmac64(a, wt[t][ks < 7 ? ks : 0], acc[t], sidx);
          else { const unsigned wa = Z_W + ((w * 4 + t) * 6 + (ks - 7) * 2) * 1024 + lane * 16; const v4u lo = lds16(wa), hi = lds16(wa + 1024);
            acc[t] = smfmac64(a, (v8u){lo.x, lo.y, lo.z, lo.w, hi.x, hi.y, hi.z, hi.w}, acc[t], sidx); }
        }
      }
#pragma unroll
      for (int t = 0; t < 4; ++t) lds4w(ub + w * 1024 + (t * 16 + n) * 16 + (2 * (g & 1) + (g >> 1)) * 4, fold_pack(acc[t]));
    }
    bar();
    if (w == 0 && s + 1 < NSEG) {
      v4u u[20], pf[20];
#pragma unroll
      for (int f = 0; f < 20; ++f) u[f] = lds16(ub + f * 1024 + lane * 16);
      for (unsigned sp = 0;; ++sp) {
        asm volatile("" ::: "memory");
        unsigned m = 0;
#pragma unroll
        for (int f = 0; f < 20; ++f) pf[f] = ld16c(rw, lane * 16, hb + ho_phys(HO_P + f * 1024));
#pragma unroll
        for (int f = 0; f < 20; ++f) m |= or4(pf[f]);
        if (__all((m & 0x80008000u) == 0) || dead) break;
        if (sp > SPINCAP) { dead = true; st4c(rw, 0, WS_CTRL + C_TMO * 256, 3000 + s); }
      }
      f4 d = {0, 0, 0, 0};
#pragma unroll
      for (int f = 0; f < 20; ++f) d = mfma32(u[f], pf[f], d);
      float o[4];
#pragma unroll
      for (int i = 0; i < 4; ++i) {
        const float t = ror8(d[i]);
        auto r2 = __builtin_amdgcn_permlane32_swap(f2u(t), f2u(t), false, false);
        o[i] = d[i] + u2f(r2[1]);
      }
      v2u mo = {pk2(o[0], o[1]), pk2(o[2], o[3])};
      if (mo.x == SENT) mo.x = 0x7FC07FC0u;
      if (mo.y == SENT) mo.y = 0x7FC07FC0u;
      const unsigned hn = WS_HO + (unsigned)(s + 1) * 65536u + (((pol >> ((s + 1) >> 5)) & 1) << 13);
      if (g < 2 && n < 8) st8c(rw, n * 16 + g * 8, hn + ho_phys(HO_MEM + c * 128), mo);
    }
    {
      const unsigned pb = WS_PX + (unsigned)s * 81920u;
      v4u px[20];
      for (unsigned sp = 0;; ++sp) {
        asm volatile("" ::: "memory");
        unsigned m = 0;
#pragma unroll
        for (int i = 0; i < 16; ++i) px[i] = ld16c(rw, lane * 16, pb + (w * 16 + i) * 1024);
#pragma unroll
        for (int i = 0; i < 4; ++i) px[16 + i] = ld16c(rw, lane * 16, pb + 65536 + (w * 4 + i) * 1024);
#pragma unroll
        for (int i = 0; i < 20; ++i) m |= or4(px[i]);
        if (__all((m & 0x80008000u) == 0) || dead) break;
        if (sp > SPINCAP) { dead = true; st4c(rw, 0, WS_CTRL + C_TMO * 256, 4000 + s); }
      }
      f4 acc[2][2];
#pragma unroll
      for (int i = 0; i < 4; ++i) acc[i >> 1][i & 1] = (f4){0, 0, 0, 0};
      const v4u um = lds16(ub + w * 1024 + lane * 16);
#pragma unroll
      for (int i = 0; i < 4; ++i) acc[i >> 1][i & 1] = mfma32(um, px[16 + i], acc[i >> 1][i & 1]);
#pragma unroll
      for (int i = 0; i < 16; ++i) acc[(i >> 1) & 1][i & 1] = mfma32(ux[i >> 2], px[i], acc[(i >> 1) & 1][i & 1]);
#pragma unroll
      for (int rq = 0; rq < 2; ++rq) {
        f4 v;
#pragma unroll
        for (int i = 0; i < 4; ++i) v[i] = (g < 2) ? acc[0][rq][i] : acc[1][rq][i];
        lds16w(Z_R + (w * 2 + rq) * 1024 + lane * 16, __builtin_bit_cast(v4u, v));
      }
      bar();
      if (w < 2) {
        float s0 = 0.f, s1 = 0.f;
#pragma unroll
        for (int q = 0; q < 8; ++q) {
          const f2 t = *(LAS f2*)(Z_R + ((q >> 1) * 2 + w) * 1024 + (((q & 1) * 2 + (g >> 1)) * 16 + n) * 16 + (g & 1) * 8);
          s0 += t[0]; s1 += t[1];
        }
        const int pi = 2 * g + (c & 1), tok = s * 32 + w * 16 + n;
        const unsigned xv = ld4p(rx, (unsigned)(tok * D_ + 16 * (c >> 1) + 2 * pi) * 2, 0);
        const unsigned hv = pk2(bflo(xv) + rbf(s0), bfhi(xv) + rbf(s1));
        const int kc = 2 * (c >> 1) + (pi >> 2);
        st4c(rw, (unsigned)(((kc & 3) * 16 + n) * 16 + (pi & 3) * 4), WS_HF + (unsigned)((s * 2 + w) * 20 + (c >> 3) * 2 + ((c >> 2) & 1)) * 1024u, hv);
      }
      bar();
    }
  }
}

__global__ void __launch_bounds__(256, 1) k_main(Params p) {
  extern __shared__ __attribute__((aligned(16))) unsigned char lds_raw[];
  unsigned char* ws = ws_base(p.ws);
  rsrc_t rw = mk_rsrc(ws, WS_END);
  const int tid = threadIdx.x, lane = tid & 63, w = __builtin_amdgcn_readfirstlane(tid >> 6);
  if (tid == 0) {
    const unsigned xcc = (unsigned)__builtin_amdgcn_s_getreg(20 | (3 << 11)) & 7u;
    unsigned v = 0;
    for (unsigned k = 0; k < 8; ++k) {
      const unsigned x = (xcc + k) & 7u;
      const unsigned rk = __hip_atomic_fetch_add((unsigned*)(ws + WS_CTRL + (C_TICKET + x) * 256), 1u, __ATOMIC_RELAXED, __HIP_MEMORY_SCOPE_AGENT) + 1u;
      if (rk < 32u) { v = rk * 8 + x; break; }
    }
    lds4w(0, v);
  }
  bar();
  const int v = __builtin_amdgcn_readfirstlane(lds4(0));
  bar();
  const int x = v & 7, idx = (v >> 3) * 4 + (x & 3);
  if (x < 4) {
    if (idx < 32) role_y(p, ws, rw, idx, lane, w);
    else if (idx < 112) role_z(p, ws, rw, idx - 32, lane, w);
    else if (idx == 112 && w == 0) {
      unsigned pol = 0;
      for (int pg = 0; pg < 2; ++pg) {
        long long diff = 0;
        for (int k = 0; k < 6; ++k)
          for (int st = 0; st < 2; ++st) {
            const unsigned char* a = ws + WS_HO + (unsigned)(pg * 32 + k) * 65536u + 49152u + st * 8192u + 8064u;
            unsigned long long t0, t1; unsigned val;
            asm volatile("s_memtime %0\n\ts_waitcnt lgkmcnt(0)\n\tglobal_load_dword %2, %3, off sc1\n\ts_waitcnt vmcnt(0)\n\ts_memtime %1\n\ts_waitcnt lgkmcnt(0)"
                         : "=&s"(t0), "=&s"(t1), "=&v"(val) : "v"(a) : "memory");
            if (k > 0) diff += st ? (long long)(t1 - t0) : -(long long)(t1 - t0);
          }
        if (diff < 0) pol |= 1u << pg;
      }
      if (lane == 0) st4c(rw, 0, WS_CTRL + C_POL * 256, pol);
    }
  } else {
    if (idx < 80) role_wu(p, ws, rw, idx, lane, w);
    else role_wqk(p, ws, rw, idx - 80, lane, w);
  }
}

__device__ __forceinline__ float gelu_t(float x) {
  const float u = 0.7978845608028654f * (x + 0.044715f * x * x * x);
  const float e = __builtin_amdgcn_exp2f(u * 2.8853900817779268f);
  return x - x * __builtin_amdgcn_rcpf(e + 1.f);
}
__device__ __forceinline__ float dot2(unsigned a, unsigned b, float c) { return __builtin_amdgcn_fdot2_f32_bf16(__builtin_bit_cast(bf2v, a), __builtin_bit_cast(bf2v, b), c, false); }

template <int STAGE>
__global__ void __launch_bounds__(256) k_mix(Params p) {
  unsigned char* ws = ws_base(p.ws);
  constexpr int KS = STAGE == 4 ? 48 : 20, NB = STAGE == 3 ? 12 : 5;
  constexpr unsigned IN = STAGE == 1 ? WS_HF : STAGE == 2 ? WS_H1F : STAGE == 3 ? WS_X1F : WS_H2F;
  constexpr unsigned WF = STAGE == 1 ? WF1 : STAGE == 2 ? WF2 : STAGE == 3 ? WF3 : WF4;
  const int tid = threadIdx.x, lane = tid & 63, w = __builtin_amdgcn_readfirstlane(tid >> 6), g = lane >> 4, n = lane & 15;
  const int s = blockIdx.x / NB, j = (blockIdx.x % NB) * 4 + w;
  rsrc_t rw = mk_rsrc(ws, WS_END);
  f4 acc[2][2]; float s1[2] = {0.f, 0.f}, s2[2] = {0.f, 0.f};
#pragma unroll
  for (int i = 0; i < 4; ++i) acc[i >> 1][i & 1] = (f4){0, 0, 0, 0};
#pragma unroll 4
  for (int ks = 0; ks < KS; ++ks) {
    v4u a[2];
#pragma unroll
    for (int rt = 0; rt < 2; ++rt) {
      if (STAGE == 2) {
        const int q = ks / 5, t = s * 32 + rt * 16 + n + (q == 0 ? -1 : q == 1 ? 1 : q == 2 ? -2 : 2);
        const unsigned o = (t >= 0 && t < S_) ? IN + (unsigned)(((t >> 4) * 20 + ks) * 64 + g * 16 + (t & 15)) * 16u : 0xF0000000u;
        a[rt] = ld16p(rw, o, 0);
      } else a[rt] = ld16p(rw, lane * 16, IN + (unsigned)((s * 2 + rt) * KS + ks) * 1024u);
    }
    const v4u w0 = ld16p(rw, lane * 16, WS_WF + WF + (unsigned)((j * KS + ks) * 2) * 1024u), w1 = ld16p(rw, lane * 16, WS_WF + WF + (unsigned)((j * KS + ks) * 2 + 1) * 1024u);
#pragma unroll
    for (int rt = 0; rt < 2; ++rt) {
      acc[rt][0] = mfma32(w0, a[rt], acc[rt][0]); acc[rt][1] = mfma32(w1, a[rt], acc[rt][1]);
      if (STAGE == 1 || STAGE == 3) {
#pragma unroll
        for (int q = 0; q < 4; ++q) { s1[rt] = dot2(a[rt][q], 0x3F803F80u, s1[rt]); s2[rt] = dot2(a[rt][q], a[rt][q], s2[rt]); }
      }
    }
  }
  if (STAGE == 2) {
#pragma unroll 4
    for (int ks = 0; ks < 20; ++ks)
#pragma unroll
      for (int rt = 0; rt < 2; ++rt) {
        const v4u a = ld16p(rw, lane * 16, WS_HF + (unsigned)((s * 2 + rt) * 20 + ks) * 1024u);
#pragma unroll
        for (int q = 0; q < 4; ++q) { s1[rt] = dot2(a[q], 0x3F803F80u, s1[rt]); s2[rt] = dot2(a[q], a[q], s2[rt]); }
      }
  }
#pragma unroll
  for (int rt = 0; rt < 2; ++rt) {
    float o[8];
#pragma unroll
    for (int i = 0; i < 4; ++i) { o[i] = acc[rt][0][i]; o[4 + i] = acc[rt][1][i]; }
    float mu = 0.f, rstd = 0.f;
    if (STAGE != 4) {
      mu = xor32_add(xor16_add(s1[rt])) * (1.f / D_);
      const float var = fmaxf(xor32_add(xor16_add(s2[rt])) * (1.f / D_) - mu * mu, 0.f);
      rstd = __builtin_amdgcn_rsqf(var + 1e-5f);
      if (STAGE == 1) rstd *= __builtin_amdgcn_rsqf(var * rstd * rstd + 1e-5f);
    }
    const unsigned fo = (unsigned)((s * 2 + rt) * 20 + j) * 1024u + lane * 16;
    if (STAGE == 1 || STAGE == 3) {
      const float* cs = (const float*)(ws + WS_CS + (STAGE == 1 ? CS1 : CS3)) + j * 32 + g * 8;
#pragma unroll
      for (int i = 0; i < 8; ++i) o[i] = gelu_t(rbf(rstd * (o[i] - mu * cs[i])));
      const v4u r = {pk2(o[0], o[1]), pk2(o[2], o[3]), pk2(o[4], o[5]), pk2(o[6], o[7])};
      if (STAGE == 1) *(v4u*)(ws + WS_H1F + fo) = r;
      else *(v4u*)(ws + WS_H2F + (unsigned)((s * 2 + rt) * 48 + j) * 1024u + lane * 16) = r;
    } else if (STAGE == 2) {
      const v4u hv = *(const v4u*)(ws + WS_HF + fo);
      v4u r;
#pragma unroll
      for (int q = 0; q < 4; ++q) r[q] = pk2(rbf((bflo(hv[q]) - mu) * rstd) + rbf(o[2 * q]), rbf((bfhi(hv[q]) - mu) * rstd) + rbf(o[2 * q + 1]));
      *(v4u*)(ws + WS_X1F + fo) = r;
    } else {
      const v4u hv = *(const v4u*)(ws + WS_HF + fo), xv = *(const v4u*)(ws + WS_X1F + fo);
      v4u r;
#pragma unroll
      for (int q = 0; q < 4; ++q) r[q] = pk2(bflo(hv[q]) + rbf(bflo(xv[q]) + rbf(o[2 * q])), bfhi(hv[q]) + rbf(bfhi(xv[q]) + rbf(o[2 * q + 1])));
      *(v4u*)(p.out + (size_t)(s * 32 + rt * 16 + n) * D_ + j * 32 + g * 8) = r;
    }
  }
}

extern "C" void kernel_launch(void* const* d_in, const int* in_sizes, int n_in, void* d_out, int out_size, void* d_ws, size_t ws_size,
                              hipStream_t stream) {
  Params p;
  p.x = (const unsigned short*)d_in[0]; p.mem0 = (const unsigned short*)d_in[1];
  p.wq = (const unsigned short*)d_in[2]; p.wk = (const unsigned short*)d_in[3]; p.wv = (const unsigned short*)d_in[4]; p.wo = (const unsigned short*)d_in[5];
  p.w1 = (const unsigned short*)d_in[8]; p.w2 = (const unsigned short*)d_in[9]; p.w3 = (const unsigned short*)d_in[10]; p.w4 = (const unsigned short*)d_in[11];
  p.out = (unsigned short*)d_out; p.ws = (unsigned char*)d_ws;
  (void)hipFuncSetAttribute((const void*)k_main, hipFuncAttributeMaxDynamicSharedMemorySize, LDS_BYTES);
  k_init<<<INIT_LN + INIT_WT + INIT_FILL, 256, 0, stream>>>(p);
  k_main<<<256, 256, LDS_BYTES, stream>>>(p);
  k_mix<1><<<64 * 5, 256, 0, stream>>>(p);
  k_mix<2><<<64 * 5, 256, 0, stream>>>(p);
  k_mix<3><<<64 * 12, 256, 0, stream>>>(p);
  k_mix<4><<<64 * 5, 256, 0, stream>>>(p);
}
```

```cpp
#include <hip/hip_runtime.h>
#include <hip/hip_bf16.h>
#include <type_traits>
#include <utility>

constexpr int S_ = 2048, D_ = 640, H_ = 32, E_ = 192, HE_ = H_ * E_, NSEG = 64, F_ = 1536;

#define LAS __attribute__((address_space(3)))
typedef unsigned v4u __attribute__((ext_vector_type(4)));
typedef unsigned v2u __attribute__((ext_vector_type(2)));
typedef unsigned v8u __attribute__((ext_vector_type(8)));
typedef float f4 __attribute__((ext_vector_type(4)));
typedef float f2 __attribute__((ext_vector_type(2)));
typedef float f16v __attribute__((ext_vector_type(16)));
typedef __bf16 bf2v __attribute__((ext_vector_type(2)));
typedef __bf16 bf8v __attribute__((ext_vector_type(8)));
typedef __bf16 bf16v __attribute__((ext_vector_type(16)));
typedef short s4v __attribute__((ext_vector_type(4)));
typedef __amdgpu_buffer_rsrc_t rsrc_t;

constexpr unsigned MB = 1u << 20;
constexpr unsigned WS_HO = 0;
constexpr unsigned WS_UM = 4 * MB;
constexpr unsigned WS_CTRL = 6 * MB;
constexpr unsigned WS_FILL_END = 6 * MB + 64 * 1024;
constexpr unsigned WS_CAL = 6 * MB + 128 * 1024;
constexpr unsigned WS_REP = 6 * MB + 256 * 1024;
constexpr unsigned WS_PX = 8 * MB;
constexpr unsigned WS_XF = 18 * MB;
constexpr unsigned WS_HF = 21 * MB;
constexpr unsigned WS_H1F = 24 * MB;
constexpr unsigned WS_X1F = 27 * MB;
constexpr unsigned WS_H2F = 30 * MB;
constexpr unsigned WS_ST = 37 * MB;
constexpr unsigned WS_QKV = 38 * MB;
constexpr unsigned WS_UX = 86 * MB;
constexpr unsigned WS_UMT = 166 * MB;
constexpr int RING = 32, W_EAGER = 2;
constexpr unsigned WS_END = 212 * MB;

constexpr unsigned WS_DBG = 211 * MB;
constexpr unsigned HO_P = 0, HO_MEM = 20480, HO_E = 30720;
#define PACE_C1 14
#define PACE_C3 8
#define PACE_UM 30
#define PACE_LATE 28
constexpr unsigned C_TICKET = 0, C_TMO = 9, C_TARM = 10, C_WFLAG = 16, C_UFLAG = 96, C_RFLAG = 128, C_TFLAG = 168, C_S1FLAG = 192, C_S2FLAG = 200, C_S3FLAG = 208;
constexpr unsigned SENT = 0xFFFFFFFFu;
constexpr unsigned SPINCAP = 1u << 18;
constexpr int LDS_BYTES = 160 * 1024;

struct Params {
  const unsigned short* x; const unsigned short* mem0;
  const unsigned short* wq; const unsigned short* wk; const unsigned short* wv; const unsigned short* wo;
  const unsigned short* w1; const unsigned short* w2; const unsigned short* w3; const unsigned short* w4;
  unsigned short* out; unsigned char* ws;
};

__device__ __forceinline__ unsigned char* ws_base(unsigned char* ws) {
  return ws + ((0u - (unsigned)(size_t)ws) & (2 * MB - 1));
}
__device__ __forceinline__ rsrc_t mk_rsrc(const void* p, unsigned bytes) {
  return __builtin_amdgcn_make_buffer_rsrc((void*)p, 0, bytes, 0x00020000);
}
__device__ __forceinline__ v4u ld16c(rsrc_t r, unsigned v, unsigned s) { return __builtin_amdgcn_raw_buffer_load_b128(r, v, s, 16); }
__device__ __forceinline__ unsigned ld4c(rsrc_t r, unsigned v, unsigned s) { return __builtin_amdgcn_raw_buffer_load_b32(r, v, s, 16); }
__device__ __forceinline__ void st16c(rsrc_t r, unsigned v, unsigned s, v4u d) { __builtin_amdgcn_raw_buffer_store_b128(d, r, v, s, 16); }
__device__ __forceinline__ void st8c(rsrc_t r, unsigned v, unsigned s, v2u d) { __builtin_amdgcn_raw_buffer_store_b64(d, r, v, s, 16); }
__device__ __forceinline__ void st4c(rsrc_t r, unsigned v, unsigned s, unsigned d) { __builtin_amdgcn_raw_buffer_store_b32(d, r, v, s, 16); }
__device__ __forceinline__ void st2c(rsrc_t r, unsigned v, unsigned s, unsigned short d) { __builtin_amdgcn_raw_buffer_store_b16(d, r, v, s, 16); }
__device__ __forceinline__ v4u ld16p(rsrc_t r, unsigned v, unsigned s) { return __builtin_amdgcn_raw_buffer_load_b128(r, v, s, 0); }
__device__ __forceinline__ unsigned ld4p(rsrc_t r, unsigned v, unsigned s) { return __builtin_amdgcn_raw_buffer_load_b32(r, v, s, 0); }

__device__ __forceinline__ unsigned f2u(float a) { return __builtin_bit_cast(unsigned, a); }
__device__ __forceinline__ float u2f(unsigned a) { return __builtin_bit_cast(float, a); }
__device__ __forceinline__ unsigned pk2(float a, float b) { bf2v t = {(__bf16)a, (__bf16)b}; return __builtin_bit_cast(unsigned, t); }
__device__ __forceinline__ unsigned pk2a(float a, float b) { unsigned r; asm("v_cvt_pk_bf16_f32 %0, |%1|, |%2|" : "=v"(r) : "v"(a), "v"(b)); return r; }
__device__ __forceinline__ float bflo(unsigned u) { return __builtin_bit_cast(float, u << 16); }
__device__ __forceinline__ float bfhi(unsigned u) { return __builtin_bit_cast(float, u & 0xFFFF0000u); }
__device__ __forceinline__ float rbf(float a) { return (float)(__bf16)a; }
__device__ __forceinline__ v4u pk8(f4 a, f4 b) { return (v4u){pk2(a[0], a[1]), pk2(a[2], a[3]), pk2(b[0], b[1]), pk2(b[2], b[3])}; }

__device__ __forceinline__ f4 mfma32(v4u a, v4u b, f4 c) {
  return __builtin_amdgcn_mfma_f32_16x16x32_bf16(__builtin_bit_cast(bf8v, a), __builtin_bit_cast(bf8v, b), c, 0, 0, 0);
}
__device__ __forceinline__ f4 smfmac64(v4u a, v8u b, f4 c, int idx) {
  return __builtin_amdgcn_smfmac_f32_16x16x64_bf16(__builtin_bit_cast(bf8v, a), __builtin_bit_cast(bf16v, b), c, idx, 0, 0);
}
template <bool FIRST, bool WA, bool WB> __device__ __forceinline__ void mfma_big(f16v& d, v4u a, v4u b) {
  if (FIRST) {
    if (WA) asm volatile("v_mfma_f32_32x32x16_bf16 %0, %1, %2, 0" : "=&v"(d) : "a"(a), "v"(b));
    else if (WB) asm volatile("v_mfma_f32_32x32x16_bf16 %0, %1, %2, 0" : "=&v"(d) : "v"(a), "a"(b));
    else asm volatile("v_mfma_f32_32x32x16_bf16 %0, %1, %2, 0" : "=&v"(d) : "v"(a), "v"(b));
  } else {
    if (WA) asm volatile("v_mfma_f32_32x32x16_bf16 %0, %1, %2, %0" : "+v"(d) : "a"(a), "v"(b));
    else if (WB) asm volatile("v_mfma_f32_32x32x16_bf16 %0, %1, %2, %0" : "+v"(d) : "v"(a), "a"(b));
    else asm volatile("v_mfma_f32_32x32x16_bf16 %0, %1, %2, %0" : "+v"(d) : "v"(a), "v"(b));
  }
}
#define PAD_BIG() do { __builtin_amdgcn_sched_barrier(0); asm volatile("s_nop 7\n\ts_nop 3"); __builtin_amdgcn_sched_barrier(0); } while (0)
__device__ __forceinline__ void mfma_v0(f4& d, v4u a, v4u b) { asm volatile("s_nop 1\n\tv_mfma_f32_16x16x32_bf16 %0, %1, %2, 0" : "=&v"(d) : "v"(a), "v"(b)); }
__device__ __forceinline__ void mfma_v(f4& d, v4u a, v4u b) { asm volatile("s_nop 1\n\tv_mfma_f32_16x16x32_bf16 %0, %1, %2, %0" : "+v"(d) : "v"(a), "v"(b)); }
__device__ __forceinline__ float vmax2(float a, float b) { float r; asm("v_max_f32 %0, %1, %2" : "=v"(r) : "v"(a), "v"(b)); return r; }
__device__ __forceinline__ float vmax3(float a, float b, float c) { float r; asm("v_max3_f32 %0, %1, %2, %3" : "=v"(r) : "v"(a), "v"(b), "v"(c)); return r; }
__device__ __forceinline__ v2u trread(unsigned lds_addr) {
  return __builtin_bit_cast(v2u, __builtin_amdgcn_ds_read_tr16_b64_v4i16((LAS s4v*)lds_addr));
}
__device__ __forceinline__ v4u lds16(unsigned a) { return *(LAS v4u*)a; }
__device__ __forceinline__ void lds16w(unsigned a, v4u v) { *(LAS v4u*)a = v; }
__device__ __forceinline__ void lds4w(unsigned a, unsigned v) { *(LAS unsigned*)a = v; }
__device__ __forceinline__ unsigned lds4(unsigned a) { return *(LAS unsigned*)a; }

__device__ __forceinline__ void glds16(const void* gsrc, unsigned dst) {
  unsigned keep;
  asm volatile("s_mov_b32 %0, m0\n\ts_mov_b32 m0, %2\n\ts_nop 0\n\tglobal_load_lds_dwordx4 %1, off\n\ts_mov_b32 m0, %0"
               : "=&s"(keep) : "v"(gsrc), "s"(dst) : "memory");
}
#define VMWAIT(n) asm volatile("s_waitcnt vmcnt(" #n ")" ::: "memory")
__device__ __forceinline__ void bar() {
  asm volatile("s_waitcnt lgkmcnt(0)" ::: "memory");
  __builtin_amdgcn_s_barrier();
  asm volatile("" ::: "memory");
}
__device__ __forceinline__ unsigned nosent(unsigned v) { return v < 0xFFFFFFFEu ? v : 0xFFFFFFFEu; }
__device__ __forceinline__ unsigned mx4(v4u v) { return max(max(v.x, v.y), max(v.z, v.w)); }
__device__ __forceinline__ unsigned or4(v4u v) { return (v.x | v.y) | (v.z | v.w); }
__device__ __forceinline__ unsigned ho_phys(unsigned o) { return ((o >> 13) << 14) | (o & 8191u); }

__device__ __forceinline__ float xor16_max(float v) { auto r = __builtin_amdgcn_permlane16_swap(f2u(v), f2u(v), false, false);
  return vmax2(u2f(r[0]), u2f(r[1])); }
__device__ __forceinline__ float xor32_max(float v) { auto r = __builtin_amdgcn_permlane32_swap(f2u(v), f2u(v), false, false);
  return vmax2(u2f(r[0]), u2f(r[1])); }
__device__ __forceinline__ float xor16_add(float v) { auto r = __builtin_amdgcn_permlane16_swap(f2u(v), f2u(v), false, false);
  return u2f(r[0]) + u2f(r[1]); }
__device__ __forceinline__ float xor32_add(float v) { auto r = __builtin_amdgcn_permlane32_swap(f2u(v), f2u(v), false, false);
  return u2f(r[0]) + u2f(r[1]); }
__device__ __forceinline__ float ror8(float v) {
  return __builtin_bit_cast(float, __builtin_amdgcn_update_dpp(0, __builtin_bit_cast(int, v), 0x128, 0xf, 0xf, true));
}
__device__ __forceinline__ void row16_max(float& a, float& b) {
  asm volatile("s_nop 1\n\t"
               "v_max_f32_dpp %0, %0, %0 row_ror:8 row_mask:0xf bank_mask:0xf\n\tv_max_f32_dpp %1, %1, %1 row_ror:8 row_mask:0xf bank_mask:0xf\n\ts_nop 0\n\t"
               "v_max_f32_dpp %0, %0, %0 row_ror:4 row_mask:0xf bank_mask:0xf\n\tv_max_f32_dpp %1, %1, %1 row_ror:4 row_mask:0xf bank_mask:0xf\n\ts_nop 0\n\t"
               "v_max_f32_dpp %0, %0, %0 row_ror:2 row_mask:0xf bank_mask:0xf\n\tv_max_f32_dpp %1, %1, %1 row_ror:2 row_mask:0xf bank_mask:0xf\n\ts_nop 0\n\t"
               "v_max_f32_dpp %0, %0, %0 row_ror:1 row_mask:0xf bank_mask:0xf\n\tv_max_f32_dpp %1, %1, %1 row_ror:1 row_mask:0xf bank_mask:0xf"
               : "+v"(a), "+v"(b));
}
__device__ __forceinline__ void row16_add(float& a, float& b) {
  asm volatile("s_nop 1\n\t"
               "v_add_f32_dpp %0, %0, %0 row_ror:8 row_mask:0xf bank_mask:0xf\n\tv_add_f32_dpp %1, %1, %1 row_ror:8 row_mask:0xf bank_mask:0xf\n\ts_nop 0\n\t"
               "v_add_f32_dpp %0, %0, %0 row_ror:4 row_mask:0xf bank_mask:0xf\n\tv_add_f32_dpp %1, %1, %1 row_ror:4 row_mask:0xf bank_mask:0xf\n\ts_nop 0\n\t"
               "v_add_f32_dpp %0, %0, %0 row_ror:2 row_mask:0xf bank_mask:0xf\n\tv_add_f32_dpp %1, %1, %1 row_ror:2 row_mask:0xf bank_mask:0xf\n\ts_nop 0\n\t"
               "v_add_f32_dpp %0, %0, %0 row_ror:1 row_mask:0xf bank_mask:0xf\n\tv_add_f32_dpp %1, %1, %1 row_ror:1 row_mask:0xf bank_mask:0xf"
               : "+v"(a), "+v"(b));
}
__device__ __forceinline__ unsigned fold_pack(f4 a) { return pk2(a[0] + a[2], a[1] + a[3]); }
__device__ __forceinline__ int sparse_idx(int lane) { return (lane & 2) ? 0xEEEE : 0x4444; }
__device__ __forceinline__ unsigned cclk() { return (unsigned)__builtin_amdgcn_s_memtime(); }
__device__ __forceinline__ unsigned rclk() { return (unsigned)__builtin_amdgcn_s_memrealtime(); }
__device__ __forceinline__ void wait_until(unsigned t) {
  unsigned d, cnt = 8192;
  asm volatile("1:\n\t"
               "s_memrealtime vcc\n\t"
               "s_sub_u32 %1, %1, 1\n\t"
               "s_cbranch_scc1 3f\n\t"
               "s_waitcnt lgkmcnt(0)\n\t"
               "s_sub_u32 %0, vcc_lo, %2\n\t"
               "s_bitcmp1_b32 %0, 23\n\t"
               "s_cbranch_scc1 1b\n\t"
               "s_and_b32 %0, %0, 0xffffff\n\t"
               "s_cmp_ge_u32 %0, 3\n\ts_cbranch_scc1 3f\n\ts_nop 3\n\t"
               "s_cmp_ge_u32 %0, 2\n\ts_cbranch_scc1 3f\n\ts_nop 3\n\t"
               "s_cmp_ge_u32 %0, 1\n\ts_cbranch_scc1 3f\n\ts_nop 10\n\t"
               "3:\n\t"
               "s_waitcnt lgkmcnt(0)"
               : "=&s"(d), "+s"(cnt) : "s"(__builtin_amdgcn_readfirstlane(t - 4u)) : "scc", "vcc", "memory");
}
__device__ __forceinline__ int lane_id() { int l; asm volatile("v_mbcnt_lo_u32_b32 %0, -1, 0\n\tv_mbcnt_hi_u32_b32 %0, -1, %0" : "=v"(l)); return l & 63; }
__device__ __forceinline__ unsigned sopaque(unsigned v) { asm volatile("" : "+s"(v)); return v; }
#define STAMP(id, slot) do {} while (0)
#define STAMPV(id, slot, v) do {} while (0)
__device__ __forceinline__ unsigned opaque(unsigned v) { asm volatile("" : "+v"(v)); return v; }
template <class F, int... I> __device__ __forceinline__ void static_for_impl(F&& f, std::integer_sequence<int, I...>) { (f(std::integral_constant<int, I>{}), ...); }
template <int N, class F> __device__ __forceinline__ void static_for(F&& f) { static_for_impl(f, std::make_integer_sequence<int, N>{}); }
#define CYC(x) do {} while (0)
#define SB() __builtin_amdgcn_sched_barrier(0)
#define PIN_A(x) asm volatile("" : "+a"(x))
__device__ __forceinline__ unsigned wait_word(rsrc_t rw, unsigned off, bool& dead) {
  unsigned v = 0;
  for (unsigned sp = 0; !dead; ++sp) {
    asm volatile("" ::: "memory");
    v = __builtin_amdgcn_readfirstlane(ld4c(rw, 0, off));
    if (v != SENT) break;
    if (sp > SPINCAP) { dead = true; st4c(rw, 0, WS_CTRL + C_TMO * 256, 5000); }
  }
  return v;
}
__device__ __forceinline__ unsigned stripe_word(rsrc_t rw, int lane) {
  int v = (int)ld4p(rw, (lane & 15) * 4, WS_CAL);
  v += __shfl_xor(v, 1); v += __shfl_xor(v, 2); v += __shfl_xor(v, 4); v += __shfl_xor(v, 8);
  return __builtin_amdgcn_readfirstlane(v) > 0 ? 13u : 2u;
}
__device__ __forceinline__ unsigned stripe(unsigned base, unsigned o, unsigned p0) {
  const unsigned t = base + ((o >> 13) << 14) + (o & 8191u);
  return t | (((p0 ^ (t >> 21)) & 1u) << 13);
}
#define HOME_QK 0
#define HOME_UX 0
__device__ __forceinline__ unsigned qk_at(int s, int h, int frag, unsigned p0) { const unsigned o = (unsigned)(((s & (RING - 1)) * 32 + h) * 24 + frag) * 1024u;
  return stripe(WS_QKV, o, HOME_QK == 2 ? p0 ^ (o >> 13) : p0 ^ HOME_QK); }
__device__ __forceinline__ unsigned ux_at(int s, int c, int h, unsigned p0) { const unsigned o = (unsigned)(((s & (RING - 1)) * 40 + c) * 32 + h) * 1024u;
  return stripe(WS_UX, o, HOME_UX == 2 ? p0 ^ (o >> 13) : p0 ^ HOME_UX); }
__device__ __forceinline__ unsigned px_at(int s, int frag, unsigned p0) { return stripe(WS_PX, (unsigned)(s * 80 + frag) * 1024u, p0); }
__device__ __forceinline__ unsigned umt_at(int s, int c, unsigned p0) { return stripe(WS_UMT, (unsigned)(s * 40 + c) * 8192u, p0); }
__device__ __forceinline__ void report(rsrc_t rw, int id, int s, unsigned cyc, unsigned p0) { st4c(rw, 0, stripe(WS_REP, id * 128u, p0), ((unsigned)s << 16) | min(cyc, 65535u)); }
__device__ __forceinline__ unsigned ho_base(int s, unsigned pol) { return WS_HO + (unsigned)s * 65536u + (((pol >> (s >> 5)) & 1u) << 13); }
__device__ __forceinline__ int wait_ge(rsrc_t rw, unsigned word, int want, bool& dead) {
  int v = -1;
  for (unsigned sp = 0; !dead; ++sp) {
    asm volatile("" ::: "memory");
    v = (int)__builtin_amdgcn_readfirstlane(ld4c(rw, 0, WS_CTRL + word * 256));
    if (v >= want) break;
    if (sp > SPINCAP) { dead = true; st4c(rw, 0, WS_CTRL + C_TMO * 256, word); }
    __builtin_amdgcn_s_sleep(2);
  }
  __builtin_amdgcn_fence(__ATOMIC_ACQUIRE, "wavefront");
  return v;
}

struct Hint {
  unsigned off; int v;
  __device__ __forceinline__ Hint(unsigned word, int cnt, int lane) : off((word + (unsigned)(lane < cnt ? lane : 0)) * 256u), v(-1) {}
  __device__ __forceinline__ void sample(rsrc_t rw) { asm volatile("" ::: "memory"); v = (int)ld4c(rw, off, WS_CTRL); }
  __device__ __forceinline__ bool has(int s) const { return __all(v >= s); }
  __device__ __forceinline__ void need(rsrc_t rw, int s, bool& dead) {
    for (unsigned sp = 0; !has(s) && !dead; ++sp) {
      if (sp) __builtin_amdgcn_s_sleep(2);
      sample(rw);
      if (sp > SPINCAP) { dead = true; st4c(rw, 0, WS_CTRL + C_TMO * 256, 7000 + s); }
    }
    __builtin_amdgcn_fence(__ATOMIC_ACQUIRE, "wavefront");
  }
};

constexpr int INIT_CAL = 16, INIT_LN = 128, INIT_FILL = 388;
__global__ void __launch_bounds__(256) k_init(Params p) {
  __shared__ float red[2][4][16];
  unsigned char* ws = ws_base(p.ws);
  const int tid = threadIdx.x, lane = tid & 63, w = tid >> 6, g = lane >> 4, n = lane & 15;
  int b = blockIdx.x;
  if (b < INIT_CAL) {
    if (w) return;
    const unsigned char* a = ws + (5 + 2 * (b & 1)) * MB + 512 * 1024 + b * 16384 + (lane & 31) * 4;
    unsigned best[2] = {~0u, ~0u};
    for (int k = 0; k < 3; ++k)
#pragma unroll
      for (int st = 0; st < 2; ++st) {
        const unsigned char* q = a + st * 8192;
        unsigned long long t0, t1; unsigned val = 0;
        asm volatile("s_memtime %0\n\ts_waitcnt lgkmcnt(0)\n\tglobal_atomic_add %2, %3, %2, off sc0\n\ts_waitcnt vmcnt(0)\n\ts_memtime %1\n\ts_waitcnt lgkmcnt(0)"
                     : "=&s"(t0), "=&s"(t1), "+v"(val) : "v"(q) : "memory");
        if (k) best[st] = min(best[st], (unsigned)(t1 - t0));
      }
    const unsigned flip = (b ^ ((unsigned)__builtin_amdgcn_s_getreg(20 | (3 << 11)) >> 2)) & 1u;
    const int dlt = (int)best[0] - (int)best[1];
    if (lane == 0) *(int*)(ws + WS_CAL + b * 4) = flip ? -dlt : dlt;
    return;
  }
  b -= INIT_CAL;
  if (b < INIT_LN) {
    const int s = b >> 1, rt = b & 1;
    const int tok = s * 32 + 16 * (n >> 3) + 8 * rt + (n & 7);
    v4u v[5]; float sum = 0.f;
#pragma unroll
    for (int k = 0; k < 5; ++k) v[k] = *(const v4u*)(p.x + (size_t)tok * D_ + (w * 5 + k) * 32 + g * 8);
#pragma unroll
    for (int k = 0; k < 5; ++k)
#pragma unroll
      for (int j = 0; j < 4; ++j) sum += bflo(v[k][j]) + bfhi(v[k][j]);
    sum = xor32_add(xor16_add(sum));
    if (g == 0) red[0][w][n] = sum;
    __syncthreads();
    const float mu = (red[0][0][n] + red[0][1][n] + red[0][2][n] + red[0][3][n]) * (1.f / D_);
    float sq = 0.f;
#pragma unroll
    for (int k = 0; k < 5; ++k)
#pragma unroll
      for (int j = 0; j < 4; ++j) { float a = bflo(v[k][j]) - mu, c = bfhi(v[k][j]) - mu; sq += a * a + c * c; }
    sq = xor32_add(xor16_add(sq));
    if (g == 0) red[1][w][n] = sq;
    __syncthreads();
    const float var = (red[1][0][n] + red[1][1][n] + red[1][2][n] + red[1][3][n]) * (1.f / D_);
    const float mub = rbf(mu), sd = rbf(sqrtf(rbf(rbf(var) + 1e-5f)));
#pragma unroll
    for (int k = 0; k < 5; ++k) {
      v4u o;
#pragma unroll
      for (int j = 0; j < 4; ++j) o[j] = pk2(rbf(bflo(v[k][j]) - mub) / sd, rbf(bfhi(v[k][j]) - mub) / sd);
      *(v4u*)(ws + WS_XF + ((size_t)(s * 20 + w * 5 + k) * 2 + rt) * 1024 + lane * 16) = o;
    }
    return;
  }
  b -= INIT_LN;
  __builtin_amdgcn_s_sleep(40);
#pragma unroll
  for (int k = 0; k < 4; ++k) {
    const unsigned o = (unsigned)b * 16384u + k * 4096u + tid * 16u;
    if (o >= WS_FILL_END || (o >= WS_UM + 80u * 16384u && o < WS_CTRL)) break;
    v4u v = {SENT, SENT, SENT, SENT};
    if (o < 65536u) {
      const unsigned lo = ((o >> 14) << 13) | (o & 8191u);
      if (lo >= HO_MEM && lo < HO_MEM + 10240u) {
        const unsigned cg = (lo - HO_MEM) >> 8, rho = (lo >> 4) & 15, i = 2 * (rho >> 2) + (rho & 1), half = (rho >> 1) & 1;
        const unsigned* m32 = (const unsigned*)p.mem0;
#pragma unroll
        for (int q = 0; q < 4; ++q) { unsigned t = m32[i * 320 + 8 * cg + 2 * q + half]; v[q] = min(t, 0xFFFFFFFEu); }
      }
    }
    *(v4u*)(ws + o) = v;
  }
}

__device__ __forceinline__ void role_wqk(const Params& p, unsigned char* ws, rsrc_t rw, int j, int lane, int w) {
  const int wu = 4 * j + w;
  const int h = wu < 192 ? wu / 6 : (wu - 192) / 3, qk = wu < 192 ? (wu % 6) / 3 : 2, e3 = wu % 3;
  const int gg = lane >> 4, hi = lane >> 5, r = (lane >> 2) & 3, cc = lane & 3;
  const int dr = lane >> 3, c16 = lane & 7, fs = ((dr >> 1) & 1);
  const unsigned short* W = (qk == 0 ? p.wq : qk == 1 ? p.wk : p.wv) + h * E_ + e3 * 64;
  const unsigned lb = w * 40960;
  auto issue = [&](int blk) {
#pragma unroll
    for (int i = 0; i < 8; ++i) {
      const int f = fs | ((i & 1) << 1);
      glds16(W + (size_t)(blk * 64 + i * 8 + dr) * HE_ + (((c16 >> 1) ^ f) * 16) + (c16 & 1) * 8, lb + (blk % 5) * 8192 + i * 1024);
    }
  };
#pragma unroll
  for (int b = 0; b < 5; ++b) issue(b);
  v4u wt[2][40];
  const int f = (r >> 1) | (hi << 1);
  const unsigned tb0 = opaque(lb + (8 * hi + r) * 128 + (((gg & 1) ^ f) * 32) + cc * 8), tb1 = opaque(lb + (8 * hi + r) * 128 + (((2 + (gg & 1)) ^ f) * 32) + cc * 8);
#pragma unroll
  for (int b = 0; b < 10; ++b) {
    if (b <= 5) VMWAIT(32); else if (b == 6) VMWAIT(24); else if (b == 7) VMWAIT(16); else if (b == 8) VMWAIT(8); else VMWAIT(0);
#pragma unroll
    for (int t = 0; t < 2; ++t)
#pragma unroll
      for (int k = 0; k < 4; ++k) { const unsigned a = (t ? tb1 : tb0) + (b % 5) * 8192 + k * 2048; v2u lo = trread(a), hi2 = trread(a + 512); wt[t][4 * b + k] = (v4u){lo.x, lo.y, hi2.x, hi2.y}; }
    asm volatile("s_waitcnt lgkmcnt(0)" ::: "memory");
    if (b + 5 < 10) issue(b + 5);
  }
#pragma unroll
  for (int k = 0; k < 32; ++k) { PIN_A(wt[0][k]); PIN_A(wt[1][k]); }
  bar();
  bool dead = false;
  const unsigned p0 = (stripe_word(rw, lane) >> 3) ^ (qk == 2 ? 1u : 0u);
  const int ofr = (qk == 1 ? 12 : 0) + e3 * 4;
  Hint done(C_TFLAG, 20, lane);
  const int flagword = C_WFLAG + j;
  const unsigned char* xsrc = ws + WS_XF + w * 10240 + lane * 16;
#pragma unroll
  for (int i = 0; i < 10; ++i) glds16(xsrc + i * 1024, w * 10240 + i * 1024);
#pragma unroll
  for (int i = 0; i < 10; ++i) glds16(xsrc + 40960 + i * 1024, 40960 + w * 10240 + i * 1024);
  VMWAIT(0);
  bar();
  const unsigned xo = (gg & 1) * 1024 + hi * 256 + (lane & 15) * 16;
  auto xoff = [](int k) { return (k >> 1) * 2048 + (k & 1) * 512; };
  v4u xr[4];
  xr[0] = lds16(xo); xr[1] = lds16(xo + xoff(1)); xr[2] = lds16(xo + xoff(2));
  f16v accA[2], accB[2];
#pragma unroll
  for (int i = 0; i < 16; ++i) { accB[0][i] = 0.f; accB[1][i] = 0.f; }
  auto put = [&](const f16v (&acc)[2], int q, int s) {
    const f16v a = acc[q >> 1]; const int o = 4 * (q & 1);
    st16c(rw, xo, qk_at(s, h, ofr, p0) + (q >> 1) * 2048 + (q & 1) * 512,
          (v4u){pk2(a[o], a[o + 1]), pk2(a[o + 2], a[o + 3]), pk2(a[o + 8], a[o + 9]), pk2(a[o + 10], a[o + 11])});
  };
  int b0 = 0;
  auto seg = [&](f16v (&acc)[2], const f16v (&prev)[2], int s) {
    const int b1 = b0 == 2 ? 0 : b0 + 1, b2 = b1 == 2 ? 0 : b1 + 1;
    const unsigned cur = opaque(xo + b0 * 40960), nxt = opaque(xo + b1 * 40960), dst2 = b2 * 40960 + w * 10240;
    b0 = b1;
    static_for<40>([&](auto KC) {
      constexpr int k = decltype(KC)::value;
      if constexpr (k == 1) {
        if (w == 0 && s > RING) done.need(rw, s - 1 - RING, dead);
        VMWAIT(0);
        bar();
        if (w == 0 && lane == 0 && s >= 2 + W_EAGER) st4c(rw, 0, WS_CTRL + flagword * 256, (unsigned)(s - 2));
        if (w == 0) { STAMP(flagword - C_WFLAG + 128, 16 + s); STAMPV(flagword - C_WFLAG + 128, 128 + s, (unsigned)__builtin_amdgcn_s_memtime()); }
      }
      mfma_big<k == 0, (k < 32), false>(acc[0], wt[0][k], xr[k & 3]);
      xr[(k + 3) & 3] = lds16(k < 37 ? cur + xoff(k + 3) : nxt + xoff(k - 37));
      SB();
      mfma_big<k == 0, (k < 32), false>(acc[1], wt[1][k], xr[k & 3]);
      SB();
      if constexpr (k >= 1 && k < 9 && (k & 1)) { if (s >= 1 + W_EAGER) put(prev, k >> 1, s - 1); }
      if constexpr (k >= 8 && k < 28 && !(k & 1)) glds16(xsrc + (size_t)(s + 2) * 40960 + ((k - 8) >> 1) * 1024, dst2 + ((k - 8) >> 1) * 1024);
      SB();
    });
    if (s < W_EAGER || s == NSEG - 1) {
      if (s >= RING) done.need(rw, s - RING, dead);
      PAD_BIG();
#pragma unroll
      for (int q = 0; q < 4; ++q) put(acc, q, s);
      VMWAIT(0);
      bar();
      if (w == 0 && lane == 0) st4c(rw, 0, WS_CTRL + flagword * 256, (unsigned)s);
    }
  };
  for (int s = 0; s < NSEG; s += 2) { seg(accA, accB, s); seg(accB, accA, s + 1); }
}

__device__ __forceinline__ constexpr int slot_grp(int j) { return j == 0 || (j >= 2 && j < 5) ? 0 : 1; }
__device__ __forceinline__ constexpr int slot_rot(int j) { return j < 2 ? 0 : (j < 5 ? j - 1 : j - 4); }
__device__ __forceinline__ int slot_ks(int j, int w) { return j < 8 ? 4 * slot_grp(j) + ((w + slot_rot(j)) & 3) : j; }
struct MemGather {
  unsigned o0, o1, o2;
  __device__ __forceinline__ MemGather(unsigned hb, int w) : o0(sopaque(hb + ho_phys(HO_MEM + w * 1024))), o1(sopaque(hb + ho_phys(HO_MEM + 4096 + w * 1024))), o2(sopaque(hb + ho_phys(HO_MEM + 8192 + w * 512))) {}
  __device__ __forceinline__ void load(rsrc_t rw, int lane, v4u& a0, v4u& a1, v2u& a2) const {
    a0 = ld16c(rw, lane * 16, o0); SB(); a1 = ld16c(rw, lane * 16, o1); SB(); a2 = __builtin_amdgcn_raw_buffer_load_b64(rw, lane * 8, o2, 16); SB();
  }
  static __device__ __forceinline__ bool bad(v4u a0, v4u a1, v2u a2) { return __any(max(max(mx4(a0), mx4(a1)), max(a2.x, a2.y)) == SENT); }
  __device__ __forceinline__ void late(rsrc_t rw, int lane, v4u& a0, v4u& a1, v2u& a2, bool& dead) const {
    for (unsigned sp = 0; !dead; ++sp) {
      asm volatile("" ::: "memory");
      load(rw, lane, a0, a1, a2);
      if (!bad(a0, a1, a2)) break;
      if (sp > SPINCAP) { dead = true; st4c(rw, 0, WS_CTRL + C_TMO * 256, 1000); }
    }
  }
};
__device__ __forceinline__ void blk_src(int h, int lane, int w, unsigned (&soff)[6], unsigned (&own)[6]) {
#pragma unroll
  for (int i = 0; i < 6; ++i) {
    const int pos = (w * 6 + i) * 64 + lane, d = pos / 24, c16 = pos % 24;
    const int et = (c16 >> 1) ^ (((d >> 1) & 1) | (((d >> 3) & 1) << 1));
    soff[i] = (unsigned)(d * HE_ + h * E_ + et * 16 + (c16 & 1) * 8); own[i] = (unsigned)(et / 3);
  }
}

constexpr unsigned Y_IMG = 122880, Y_KX = Y_IMG + 6144, Y_M = Y_KX + 12288, Y_QX = Y_M + 10240;
__device__ __forceinline__ constexpr int y_blk_qk(int i) { return i < 15 ? (i < 8 ? 0 : 1) : ((i - 15) & 1 ? 0 : 1); }
__device__ __forceinline__ constexpr int y_blk_ks(int i) {
  constexpr int qs[8] = {0, 1, 3, 4, 5, 7, 8, 9};
  constexpr int kk[7] = {1, 2, 3, 5, 6, 7, 9};
  return i < 8 ? qs[i] : (i < 15 ? kk[i - 8] : 2 * (i - 15));
}
__device__ __forceinline__ constexpr int y_tail_t(int j) { constexpr int o[24] = {0, 1, 0, 1, 0, 2, 0, 2, 1, 3, 1, 3, 2, 4, 2, 4, 3, 5, 3, 5, 4, 5, 4, 5}; return o[j]; }
__device__ __forceinline__ constexpr int y_tail_k(int j) { int c = 0; for (int i = 0; i < j; ++i) if (y_tail_t(i) == y_tail_t(j)) ++c; return 6 + c; }
__device__ __forceinline__ constexpr int y_find(int qk, int ks) { for (int i = 0; i < 20; ++i) if (y_blk_qk(i) == qk && y_blk_ks(i) == ks) return i; return -1; }

__device__ __forceinline__ void role_y(const Params& p, unsigned char* ws, rsrc_t rw, int h, int lane, int w) {
  const int g = lane >> 4, n = lane & 15, r = (lane >> 2) & 3, cc = lane & 3;
  bool dead = false;
  unsigned soff[6], own[6];
  blk_src(h, lane, w, soff, own);
  auto issue = [&](int i) {
    const unsigned short* W = y_blk_qk(i) ? p.wk : p.wq;
#pragma unroll
    for (int q = 0; q < 6; ++q) glds16(W + soff[q] + (unsigned)slot_ks(y_blk_ks(i), own[q]) * (64u * HE_), (i % 5) * 24576 + (w * 6 + q) * 1024);
  };
  unsigned tb[3];
  { const int f = (r >> 1) | ((g & 1) << 1);
#pragma unroll
    for (int t = 0; t < 3; ++t) tb[t] = (8 * g + r) * 384 + (((3 * w + t) ^ f) * 32) + cc * 8; }
  auto rdfrag = [&](int slot, int t) -> v8u {
    const unsigned a = tb[t] + slot * 24576;
    v2u q0 = trread(a), q1 = trread(a + 4 * 384), q2 = trread(a + 32 * 384), q3 = trread(a + 36 * 384);
    return (v8u){q0.x, q0.y, q1.x, q1.y, q2.x, q2.y, q3.x, q3.y};
  };
#pragma unroll
  for (int i = 0; i < 5; ++i) issue(i);
  v8u wt[15][3];
#pragma unroll
  for (int i = 0; i < 15; ++i) {
    VMWAIT(24);
    bar();
#pragma unroll
    for (int t = 0; t < 3; ++t) wt[i][t] = rdfrag(i % 5, t);
    bar();
#pragma unroll
    for (int t = 0; t < 3; ++t) if (i * 3 + t < 32) PIN_A(wt[i][t]);
    issue(i + 5);
  }
  VMWAIT(0);
  const unsigned pol = stripe_word(rw, lane);
  const unsigned p0 = pol >> 3;
  if (w == 1) wait_ge(rw, C_TARM, 19, dead);
  const int jf0 = C_WFLAG + (6 * h) / 4;
  int seen = -1;
  auto need_qk = [&](int s) {
    for (unsigned sp = 0; seen < s && !dead; ++sp) {
      asm volatile("" ::: "memory");
      const int v = (int)ld4c(rw, (lane & 1) * 256, WS_CTRL + jf0 * 256);
      seen = min(__builtin_amdgcn_readlane(v, 0), __builtin_amdgcn_readlane(v, 1));
      if (sp > SPINCAP) { dead = true; st4c(rw, 0, WS_CTRL + C_TMO * 256, 1500 + s); }
      if (seen < s) __builtin_amdgcn_s_sleep(2);
    }
    __builtin_amdgcn_fence(__ATOMIC_ACQUIRE, "wavefront");
  };
  const unsigned kxb = opaque(Y_KX + lane * 16);
  const unsigned kxp = opaque(Y_KX + (g * 16 + (n & 8) + 4 * (n & 1) + 2 * ((n >> 2) & 1) + ((n >> 1) & 1)) * 16);
  auto load_kx = [&](int s, v4u (&t)[12]) {
#pragma unroll
    for (int i = 0; i < 12; ++i) t[i] = ld16c(rw, lane * 16, qk_at(s, h, 12 + (i & ~3), p0) + (i & 3) * 1024);
  };
  const unsigned qxb = opaque(Y_QX + ((w - 1) & 1) * 6144 + lane * 16);
  auto load_qx = [&](int s, v4u (&q)[6]) {
#pragma unroll
    for (int u = 0; u < 6; ++u) q[u] = ld16c(rw, (n >> 3) * 1024 + (g * 16 + 8 * (w - 1) + (n & 7)) * 16, qk_at(s, h, 4 * (u >> 1), p0) + (u & 1) * 2048);
  };
  if (w == 3) need_qk(1);
  bar();
  const int sidx = sparse_idx(lane);
  const float cexp = 0.07216878364870322f * 1.4426950408889634f;
  const unsigned imwk = opaque(Y_IMG + 3072 + 3 * w * 256 + n * 16 + g * 4);
  unsigned imwq[3];
#pragma unroll
  for (int t = 0; t < 3; ++t) { const int et = 3 * w + t;
    imwq[t] = opaque(Y_IMG + (et >> 2) * 1024 + ((2 * ((et >> 1) & 1) + (n >> 3)) * 16 + 4 * g + 2 * ((n >> 1) & 1)) * 16 + (2 * ((n >> 2) & 1) + (et & 1)) * 4 + (n & 1) * 2); }
  const unsigned imr = opaque(Y_IMG + 3072 + (4 * g + r) * 16 + (cc & 1) * 8), imq = opaque(Y_IMG + lane * 16);
  const unsigned mw = opaque(Y_M + w * 1024 + lane * 16), mw2 = opaque(Y_M + 8192 + w * 512 + lane * 8);
  unsigned mr[4];
#pragma unroll
  for (int i = 1; i < 4; ++i) mr[i] = opaque(Y_M + ((w + i) & 3) * 1024 + lane * 16);
  mr[0] = opaque(Y_M + 8192 + lane * 16);
  unsigned tb2[3];
#pragma unroll
  for (int t = 0; t < 3; ++t) tb2[t] = opaque(tb[t] + 3 * 24576);
  STAMP(h, 1);
  STAMPV(h, 2, (unsigned)__builtin_amdgcn_s_memtime());
  unsigned tpass = rclk();
  bool first = true; unsigned c0 = 0, tb0 = 0, cy[8] = {0, 0, 0, 0, 0, 0, 0, 0}; (void)tb0; (void)first;
  auto next_pace = [&]() {
    const unsigned e = __builtin_amdgcn_readfirstlane(lds4(Y_M + w * 1024)); tpass = (e >> 8) + (e & 255u); };
  auto proj = [&](auto FIN, unsigned hb, v4u (&kx)[12], auto&& before, auto&& shadow) {
    constexpr bool fin = decltype(FIN)::value;
    v4u a[10]; v2u a2; f4 acc[6]; v2u sa[3][4], sb[3][4];
    auto cat = [](const v2u (&q)[4]) -> v8u { return (v8u){q[0].x, q[0].y, q[1].x, q[1].y, q[2].x, q[2].y, q[3].x, q[3].y}; };
    auto rdq = [&](int slot, int t, int q) -> v2u {
      return trread((slot < 3 ? tb[t] + slot * 24576 : tb2[t] + (slot - 3) * 24576) + (32 * (q >> 1) + 4 * (q & 1)) * 384); };
    auto rda = [&](int j) -> v4u { return j < 8 ? lds16(mr[slot_rot(j)] + slot_grp(j) * 4096) : lds16(mr[0] + (j - 8) * 1024); };
    auto zero = [&]() {
#pragma unroll
      for (int t = 0; t < 6; ++t) { acc[t] = (f4){0, 0, 0, 0}; asm volatile("" : "+v"(acc[t])); } };
    auto mm = [&](auto GC) {
      constexpr int G = decltype(GC)::value;
      constexpr int ks = G < 36 ? G / 6 : y_tail_k(G < 36 ? 0 : G - 36), t = G < 36 ? G % 6 : y_tail_t(G < 36 ? 0 : G - 36);
      constexpr int bi = y_find(t / 3, ks);
      if constexpr (bi < 15) acc[t] = smfmac64(a[ks], wt[bi][t % 3], acc[t], sidx);
      else acc[t] = smfmac64(a[ks], t < 3 ? cat(sb[t % 3]) : cat(sa[t % 3]), acc[t], sidx);
      SB();
    };
    before();
    SB();
#pragma unroll
    for (int i = 0; i < 12; ++i) sa[i >> 2][i & 3] = rdq(0, i >> 2, i & 3);
    zero();
    const MemGather mg(hb, w);
    SB();
    wait_until(tpass);
    CYC(cy[0]);
    SB();
    mg.load(rw, lane, a[0], a[1], a2);
    c0 = cclk();
    shadow();
    SB();
#pragma unroll
    for (int i = 0; i < 12; ++i) sb[i >> 2][i & 3] = rdq(1, i >> 2, i & 3);
    SB();
    lds16w(mw, a[0]);
    SB();
    mm(std::integral_constant<int, 0>{});
    lds16w(mw + 4096, a[1]);
    SB();
    mm(std::integral_constant<int, 1>{});
    *(LAS v2u*)mw2 = a2;
    SB();
    mm(std::integral_constant<int, 2>{});
    unsigned mx = max(mx4(a[0]), a2.x); asm volatile("" : "+v"(mx)); SB();
    mm(std::integral_constant<int, 3>{});
    mx = max(max(mx, a2.y), mx4(a[1])); asm volatile("" : "+v"(mx)); SB();
    mm(std::integral_constant<int, 4>{});
    first = !__any(mx == SENT);
    mm(std::integral_constant<int, 5>{});
    if (__builtin_expect(!first, 0)) {
      mg.late(rw, lane, a[0], a[1], a2, dead);
      lds16w(mw, a[0]); lds16w(mw + 4096, a[1]); *(LAS v2u*)mw2 = a2;
      zero();
      static_for<6>(mm);
    }
    CYC(cy[1]);
    bar();
    CYC(cy[2]);
    SB();
    f2 fs = {0.f, 0.f}; unsigned f0 = 0;
    static_for<54>([&](auto GC0) {
      constexpr int G = decltype(GC0)::value + 6;
      if constexpr (G < 14) a[G - 4] = rda(G - 4);
      else if constexpr (G < 24) { sa[(G - 14) >> 2][(G - 14) & 3] = rdq(2, (G - 14) >> 2, (G - 14) & 3); if constexpr (G >= 22) sa[2][G - 20] = rdq(2, 2, G - 20); }
      else if constexpr (G < 36) sb[(G - 24) >> 2][(G - 24) & 3] = rdq(3, (G - 24) >> 2, (G - 24) & 3);
      else if constexpr (G < 48) sa[(G - 36) >> 2][(G - 36) & 3] = rdq(4, (G - 36) >> 2, (G - 36) & 3);
      else if constexpr (fin) kx[G - 48] = lds16(kxp + (G - 48) * 1024);
      if constexpr (G >= 45) {
        constexpr int tp = (G - 45) / 4, ph = (G - 45) % 4;
        if constexpr (ph == 0) { fs = (f2){acc[tp][0], acc[tp][1]} + (f2){acc[tp][2], acc[tp][3]}; asm volatile("" : "+v"(fs)); }
        if constexpr (ph == 1) { f0 = pk2(fs[0], fs[1]); asm volatile("" : "+v"(f0)); }
        if constexpr (ph == 2) { if constexpr (tp < 3) *(LAS unsigned short*)imwq[tp < 3 ? tp : 0] = (unsigned short)f0; else lds4w(imwk, f0); }
        if constexpr (ph == 3 && tp < 3) *(LAS unsigned short*)(imwq[tp < 3 ? tp : 0] + 16) = (unsigned short)(f0 >> 16);
      }
      SB();
      mm(std::integral_constant<int, G>{});
    });
    CYC(cy[3]);
    lds4w(imwk + 256, fold_pack(acc[4]));
    lds4w(imwk + 512, fold_pack(acc[5]));
    bar();
  };
  if (w == 0) {
    const unsigned pso = (n >> 3) * 256 + (4 * g + 2 * ((n >> 1) & 1) + ((n >> 2) & 1)) * 16 + (n & 1) * 8, pmo = (4 * g + 2 * ((n >> 1) & 1) + (n >> 3)) * 8 + ((n >> 2) & 1) * 4 + (n & 1) * 2;
    for (int s = 0; s < NSEG; ++s) {
      const unsigned hb = ho_base(s, pol);
      const unsigned ox = sopaque(hb + ho_phys(HO_P + (32 + 4 * h) * 128)), om = sopaque(hb + ho_phys(HO_P + h * 128));
      v4u kx[12];
      f4 d0 = {0, 0, 0, 0}, d1 = d0, d2 = d0;
      proj(std::true_type{}, hb, kx, []() {}, []() {});
      CYC(cy[4]);
      v4u qa[3]; v2u kr[12];
#pragma unroll
      for (int v = 0; v < 3; ++v) qa[v] = lds16(imq + v * 1024);
#pragma unroll
      for (int i = 0; i < 12; ++i) kr[i] = trread(imr + i * 256);
      asm volatile("" : "+v"(d0), "+v"(d1), "+v"(d2));
      SB();
#pragma unroll
      for (int v = 0; v < 3; ++v) {
        const v4u l0 = kx[4 * v], h0 = kx[4 * v + 2], l1 = kx[4 * v + 1], h1 = kx[4 * v + 3];
        d1 = smfmac64(qa[v], (v8u){l0.x, l0.y, l0.z, l0.w, h0.x, h0.y, h0.z, h0.w}, d1, sidx);
        d2 = smfmac64(qa[v], (v8u){l1.x, l1.y, l1.z, l1.w, h1.x, h1.y, h1.z, h1.w}, d2, sidx);
        SB();
      }
#pragma unroll
      for (int v = 0; v < 3; ++v)
        d0 = smfmac64(qa[v], (v8u){kr[4 * v].x, kr[4 * v].y, kr[4 * v + 1].x, kr[4 * v + 1].y, kr[4 * v + 2].x, kr[4 * v + 2].y, kr[4 * v + 3].x, kr[4 * v + 3].y}, d0, sidx);
      SB();
      CYC(cy[5]);
      const f2 x0 = (f2){d1[0], d1[1]} + (f2){d1[2], d1[3]}, x1 = (f2){d2[0], d2[1]} + (f2){d2[2], d2[3]}, xm = (f2){d0[0], d0[1]} + (f2){d0[2], d0[3]};
      float ma = vmax3(x0[0], x1[0], xm[0]), mb = vmax3(x0[1], x1[1], xm[1]);
      row16_max(ma, mb);
      const float na = -ma * cexp, nb = -mb * cexp;
      const float e0a = __builtin_amdgcn_exp2f(__builtin_fmaf(x0[0], cexp, na)), e0b = __builtin_amdgcn_exp2f(__builtin_fmaf(x0[1], cexp, nb));
      const float e1a = __builtin_amdgcn_exp2f(__builtin_fmaf(x1[0], cexp, na)), e1b = __builtin_amdgcn_exp2f(__builtin_fmaf(x1[1], cexp, nb));
      const float ema = __builtin_amdgcn_exp2f(__builtin_fmaf(xm[0], cexp, na)), emb = __builtin_amdgcn_exp2f(__builtin_fmaf(xm[1], cexp, nb));
      float sa_ = __builtin_fmaf(ema, 0.5f, e0a + e1a), sb_ = __builtin_fmaf(emb, 0.5f, e0b + e1b);
      const float ems = (n & 8) ? emb : ema;
      row16_add(sa_, sb_);
      const float ra = __builtin_amdgcn_rcpf(sa_), rb = __builtin_amdgcn_rcpf(sb_);
      {
        const unsigned pa = pk2a(e0a * ra, e1a * ra), pb2 = pk2a(e0b * rb, e1b * rb);
        const unsigned lo = (unsigned)__builtin_amdgcn_update_dpp((int)pa, (int)pb2, 0x114, 0xf, 0xa, false);
        const unsigned hi = (unsigned)__builtin_amdgcn_update_dpp((int)pb2, (int)pa, 0x104, 0xf, 0x5, false);
        st8c(rw, pso, ox, (v2u){lo, hi});
      }
      st2c(rw, pmo, om, (unsigned short)pk2a(ems * ((n & 8) ? rb : ra), 0.f));
      SB();
      if (lane == 0) report(rw, h, s, cclk() - c0, p0);
      CYC(cy[6]);
      STAMP(h, 16 + s * 8 + 4);
      bar();
      next_pace();
    }
  } else if (w == 3) {
    v4u t[12];
    load_kx(0, t);
    for (int s = 0; s < NSEG; ++s) {
      const unsigned hb = ho_base(s, pol);
      proj(std::false_type{}, hb, t, [&]() {
#pragma unroll
        for (int i = 0; i < 12; ++i) lds16w(kxb + i * 1024, t[i]); }, []() {});
      unsigned e = ld4c(rw, 0, hb + ho_phys(HO_E));
      SB();
      load_kx(s + 1 < NSEG ? s + 1 : s, t);
      SB();
      e = __builtin_amdgcn_readfirstlane(e);
      if (e == SENT && s + 1 < NSEG) e = wait_word(rw, hb + ho_phys(HO_E), dead);
      if (lane < 4) lds4w(Y_M + lane * 1024, e);
      STAMP(h, 16 + s * 8 + 3); STAMPV(h, 16 + s * 8 + 7, first ? 0 : 1); STAMPV(h, 16 + s * 8, tpass);
      bar();
      next_pace();
      need_qk(s + 2 < NSEG ? s + 2 : NSEG - 1);
    }
  } else {
    const int rq = w - 1, tok = 16 * rq + n;
    v4u qn[6];
    load_qx(0, qn);
    for (int s = 0; s < NSEG; ++s) {
      const unsigned hb = ho_base(s, pol);
      v4u t[12];
      proj(std::false_type{}, hb, t, []() {}, [&]() {
#pragma unroll
        for (int u = 0; u < 6; ++u) lds16w(qxb + u * 1024, qn[u]); });
      __builtin_amdgcn_s_sleep(2);
      f4 d0, d1, d2;
      {
        v4u qf[6]; v2u kr[12];
#pragma unroll
        for (int u = 0; u < 6; ++u) { qf[u] = lds16(qxb + u * 1024); t[2 * u] = lds16(kxb + 2 * u * 1024); t[2 * u + 1] = lds16(kxb + (2 * u + 1) * 1024); }
#pragma unroll
        for (int u = 0; u < 6; ++u) {
          if (u == 0) { mfma_v0(d1, t[0], qf[0]); mfma_v0(d2, t[1], qf[0]); } else { mfma_v(d1, t[2 * u], qf[u]); mfma_v(d2, t[2 * u + 1], qf[u]); }
          kr[2 * u] = trread(imr + 2 * u * 256); kr[2 * u + 1] = trread(imr + (2 * u + 1) * 256);
          SB();
        }
#pragma unroll
        for (int u = 0; u < 6; ++u) {
          const v4u kf = {kr[2 * u].x, kr[2 * u].y, kr[2 * u + 1].x, kr[2 * u + 1].y};
          if (u == 0) mfma_v0(d0, kf, qf[0]); else mfma_v(d0, kf, qf[u]);
        }
        asm volatile("s_nop 7" : "+v"(d0), "+v"(d1), "+v"(d2));
      }
      bar();
      load_qx(s + 1 < NSEG ? s + 1 : s, qn);
      SB();
      next_pace();
      float v[10];
      const float NEG = -1.0e9f * 1.4426950408889634f;
#pragma unroll
      for (int i = 0; i < 4; ++i) { const int key = 16 * (g >> 1) + 4 * (g & 1) + i; v[i] = (key <= tok) ? d1[i] * cexp : NEG; v[4 + i] = (key + 8 <= tok) ? d2[i] * cexp : NEG; }
      v[8] = ((g & 2) ? d0[2] : d0[0]) * cexp; v[9] = ((g & 2) ? d0[3] : d0[1]) * cexp;
      float m = vmax3(vmax3(v[0], v[1], v[2]), vmax3(v[3], v[4], v[5]), vmax3(vmax3(v[6], v[7], v[8]), v[9], v[9]));
      m = xor32_max(xor16_max(m));
      float sum = 0.f;
#pragma unroll
      for (int i = 0; i < 10; ++i) { v[i] = __builtin_amdgcn_exp2f(v[i] - m); sum += v[i]; }
      sum = xor32_add(xor16_add(sum));
      const float rs = __builtin_amdgcn_rcpf(sum);
      v4u px;
#pragma unroll
      for (int i = 0; i < 4; ++i) px[i] = pk2a(v[i] * rs, v[4 + i] * rs);
      if (rq || g < 2) st16c(rw, lane * 16, px_at(s, h * 2 + rq, p0), px);
      st4c(rw, ((h & 3) * 16 + n) * 16 + (2 * (g & 1) + (g >> 1)) * 4, px_at(s, (32 + (h >> 2)) * 2 + rq, p0), pk2a(v[8] * rs, v[9] * rs));
      STAMP(h, 16 + s * 8 + 4 + w);
    }
  }
  STAMP(h, 4);
  STAMPV(h, 3, (unsigned)__builtin_amdgcn_s_memtime());
}

template <bool PERM>
__device__ __forceinline__ void load_wo(const Params& p, int h, int lane, int w, v4u (&wo)[10][6]) {
  const int g = lane >> 4, r = (lane >> 2) & 3, cc = lane & 3;
  const unsigned lb = w * 30720;
  auto issue = [&](int u) {
#pragma unroll
    for (int i = 0; i < 10; ++i) {
      const int pos = i * 64 + lane, e = pos / 20, c16 = pos % 20;
      glds16(p.wo + (size_t)(h * E_ + u * 32 + e) * D_ + w * 160 + c16 * 8, lb + (u % 3) * 10240 + i * 1024);
    }
  };
  issue(0); issue(1); issue(2);
  const unsigned base = opaque(lb + (PERM ? 4 * g + r : 8 * g + r) * 320 + cc * 8);
#pragma unroll
  for (int u = 0; u < 6; ++u) {
    if (u <= 3) VMWAIT(20); else if (u == 4) VMWAIT(10); else VMWAIT(0);
#pragma unroll
    for (int t = 0; t < 10; ++t) {
      const unsigned a = base + (u % 3) * 10240 + t * 32;
      v2u lo = trread(a), hi = trread(a + (PERM ? 16 : 4) * 320);
      wo[t][u] = (v4u){lo.x, lo.y, hi.x, hi.y};
    }
    asm volatile("s_waitcnt lgkmcnt(0)" ::: "memory");
    if (u + 3 < 6) issue(u + 3);
  }
}

__device__ __forceinline__ void role_v2(const Params& p, unsigned char* ws, rsrc_t rw, int h, int lane, int w) {
  const int gg = lane >> 4, hi = lane >> 5, r = (lane >> 2) & 3, cc = lane & 3;
  v4u wo[5][12];
  {
    const unsigned lb = w * 30720;
    auto issue = [&](int u) {
#pragma unroll
      for (int i = 0; i < 10; ++i) {
        const int pos = i * 64 + lane, e = pos / 20, c16 = pos % 20;
        glds16(p.wo + (size_t)(h * E_ + u * 32 + e) * D_ + w * 160 + c16 * 8, lb + (u % 3) * 10240 + i * 1024);
      }
    };
    issue(0); issue(1); issue(2);
    const unsigned base = opaque(lb + (4 * hi + r) * 320 + (gg & 1) * 32 + cc * 8);
#pragma unroll
    for (int u = 0; u < 6; ++u) {
      if (u <= 3) VMWAIT(20); else if (u == 4) VMWAIT(10); else VMWAIT(0);
#pragma unroll
      for (int t = 0; t < 5; ++t)
#pragma unroll
        for (int k = 0; k < 2; ++k) { const unsigned a = base + (u % 3) * 10240 + t * 64 + k * 8 * 320; v2u lo = trread(a), hi2 = trread(a + 16 * 320); wo[t][2 * u + k] = (v4u){lo.x, lo.y, hi2.x, hi2.y}; }
      asm volatile("s_waitcnt lgkmcnt(0)" ::: "memory");
      if (u + 3 < 6) issue(u + 3);
    }
  }
#pragma unroll
  for (int t = 0; t < 5; ++t)
#pragma unroll
    for (int k = 0; k < 12; ++k) PIN_A(wo[t][k]);
  bar();
  bool dead = false;
  const int jf0 = C_WFLAG + 48 + (3 * h) / 4, jf1 = C_WFLAG + 48 + (3 * h + 2) / 4;
  const unsigned fo = ((lane & 1) ? jf1 : jf0) * 256u;
  int fl = -1;
  auto need = [&](int s) {
    for (unsigned sp = 0; !__all(fl >= s) && !dead; ++sp) {
      asm volatile("" ::: "memory");
      if (sp) __builtin_amdgcn_s_sleep(2);
      fl = (int)ld4c(rw, fo, WS_CTRL);
      if (sp > SPINCAP) { dead = true; st4c(rw, 0, WS_CTRL + C_TMO * 256, 2500 + s); }
    }
    __builtin_amdgcn_fence(__ATOMIC_ACQUIRE, "wavefront");
  };
  const unsigned l16 = lane * 16u, vw = opaque(w * 3072 + l16), vr = opaque((gg & 1) * 1024 + hi * 256 + (lane & 15) * 16);
  const unsigned uo = (gg & 1) * 65536u + hi * 256 + (lane & 15) * 16;
  v4u vn[3], va[12], oa[10], ob[10];
  const unsigned p0 = stripe_word(rw, lane) >> 3;
  auto ldv = [&](int s, int i) { vn[i] = ld16c(rw, l16, qk_at(s, h, 3 * w + i, p0 ^ 1u)); };
  auto put = [&](const v4u (&o)[10], int q, int s) { st16c(rw, uo, ux_at(s, w * 10 + (q & ~1), h, p0) + (q & 1) * 512, o[q]); };
#pragma unroll
  for (int t = 0; t < 10; ++t) ob[t] = (v4u){0, 0, 0, 0};
  auto fetch = [&](int s) {
    need(s);
#pragma unroll
    for (int i = 0; i < 3; ++i) ldv(s, i);
#pragma unroll
    for (int i = 0; i < 3; ++i) lds16w(vw + i * 1024, vn[i]);
    bar();
#pragma unroll
    for (int k = 0; k < 12; ++k) va[k] = lds16(vr + (k >> 1) * 2048 + (k & 1) * 512);
    bar();
  };
  fetch(0);
  auto seg = [&](v4u (&out)[10], const v4u (&prev)[10], int s) {
    if (w == 0) { STAMP(200 + h, 16 + s); STAMPV(200 + h, 128 + s, (unsigned)__builtin_amdgcn_s_memtime()); }
    const int sn = s + 1 < NSEG ? s + 1 : s;
    f16v acc[2];
    auto pack = [&](int t, int q) { const f16v a = acc[t & 1]; const int o = 4 * q;
      out[2 * t + q] = (v4u){pk2(a[o], a[o + 8]), pk2(a[o + 1], a[o + 9]), pk2(a[o + 2], a[o + 10]), pk2(a[o + 3], a[o + 11])}; asm volatile("" : "+v"(out[2 * t + q])); };
    static_for<60>([&](auto GC) {
      constexpr int G = decltype(GC)::value, t = G / 12, k = G % 12;
      if constexpr (G == 42) { if (s > 0) { need(sn); ldv(sn, 0); ldv(sn, 1); ldv(sn, 2); } }
      mfma_big<k == 0, false, true>(acc[t & 1], va[k], wo[t][k]);
      SB();
      if constexpr (t >= 1 && (k == 3 || k == 5)) pack(t - 1, k == 5);
      if constexpr (G < 30 && G % 3 == 0) { if (s >= 2) put(prev, G / 3, s - 1); }
      if constexpr (G == 2) { if (!__all(fl >= sn)) fl = (int)ld4c(rw, fo, WS_CTRL); }
      SB();
    });
    VMWAIT(0);
#pragma unroll
    for (int i = 0; i < 3; ++i) lds16w(vw + (sn & 1) * 12288 + i * 1024, vn[i]);
    bar();
    if (w == 0 && lane == 0 && s >= 2) st4c(rw, 0, WS_CTRL + (C_UFLAG + h) * 256, (unsigned)(s - 1));
#pragma unroll
    for (int k = 0; k < 12; ++k) va[k] = lds16(vr + (sn & 1) * 12288 + (k >> 1) * 2048 + (k & 1) * 512);
    PAD_BIG();
    pack(4, 0); pack(4, 1);
    if (s == 0 || s == NSEG - 1) {
#pragma unroll
      for (int t = 0; t < 10; ++t) put(out, t, s);
      VMWAIT(0);
      bar();
      if (w == 0 && lane == 0) st4c(rw, 0, WS_CTRL + (C_UFLAG + h) * 256, (unsigned)s);
      if (s == 0) fetch(1);
    }
  };
  for (int s = 0; s < NSEG; s += 2) { seg(oa, ob, s); seg(ob, oa, s + 1); }
}

constexpr unsigned V_IMG = 122880, V_M = V_IMG + 3072, V_T = V_M + 10240;
__device__ __forceinline__ void role_v(const Params& p, unsigned char* ws, rsrc_t rw, int h, int lane, int w) {
  const int g = lane >> 4, n = lane & 15, r = (lane >> 2) & 3, cc = lane & 3;
  bool dead = false;
  v4u wo[10][6];
  load_wo<false>(p, h, lane, w, wo);
  v8u w2[10][3];
#pragma unroll
  for (int t = 0; t < 10; ++t)
#pragma unroll
    for (int k = 0; k < 3; ++k) { const v4u lo = wo[t][2 * k], hi = wo[t][2 * k + 1]; w2[t][k] = (v8u){lo.x, lo.y, lo.z, lo.w, hi.x, hi.y, hi.z, hi.w}; PIN_A(w2[t][k]); }
  bar();
  unsigned soff[6], own[6];
  blk_src(h, lane, w, soff, own);
  auto issue = [&](int i) {
#pragma unroll
    for (int q = 0; q < 6; ++q) glds16(p.wv + soff[q] + (unsigned)slot_ks(i < 5 ? 2 * i + 1 : 2 * (i - 5), own[q]) * (64u * HE_), (i % 5) * 24576 + (w * 6 + q) * 1024);
  };
  unsigned tb[3], tb2[3];
  { const int f = (r >> 1) | ((g & 1) << 1);
#pragma unroll
    for (int t = 0; t < 3; ++t) { tb[t] = opaque((8 * g + r) * 384 + (((3 * w + t) ^ f) * 32) + cc * 8); tb2[t] = opaque(tb[t] + 3 * 24576); } }
  auto rdq = [&](int slot, int t, int q) -> v2u {
    return trread((slot < 3 ? tb[t] + slot * 24576 : tb2[t] + (slot - 3) * 24576) + (32 * (q >> 1) + 4 * (q & 1)) * 384); };
  auto cat = [](const v2u (&q)[4]) -> v8u { return (v8u){q[0].x, q[0].y, q[1].x, q[1].y, q[2].x, q[2].y, q[3].x, q[3].y}; };
#pragma unroll
  for (int i = 0; i < 5; ++i) issue(i);
  v8u w1[5][3];
#pragma unroll
  for (int i = 0; i < 5; ++i) {
    VMWAIT(24);
    bar();
#pragma unroll
    for (int t = 0; t < 3; ++t) { v2u q[4];
#pragma unroll
      for (int k = 0; k < 4; ++k) q[k] = rdq(i, t, k);
      w1[i][t] = cat(q); }
    bar();
    if (i == 0) { PIN_A(w1[0][0]); PIN_A(w1[0][1]); }
    issue(i + 5);
  }
  VMWAIT(0);
  const unsigned pol = stripe_word(rw, lane);
  bar();
  const int sidx = sparse_idx(lane);
  const unsigned imw = opaque(V_IMG + (4 * g + 2 * ((n >> 1) & 1)) * 16 + (n >> 2) * 4 + (n & 1) * 2 + 3 * w * 256), imr = opaque(V_IMG + lane * 16);
  const unsigned mw = opaque(V_M + w * 1024 + lane * 16), mw2 = opaque(V_M + 8192 + w * 512 + lane * 8);
  unsigned mr[4];
#pragma unroll
  for (int i = 1; i < 4; ++i) mr[i] = opaque(V_M + ((w + i) & 3) * 1024 + lane * 16);
  mr[0] = opaque(V_M + 8192 + lane * 16);
  const unsigned umo = h * 256 + n * 16 + g * 16384, umo2 = g >= 2 ? umo : 0xF0000000u;
  STAMP(32 + h, 1);
  unsigned tpass = rclk();
  for (int s = 0; s < NSEG; ++s) {
    const unsigned hb = ho_base(s, pol);
    bool first; unsigned cy[8] = {0, 0, 0, 0, 0, 0, 0, 0}; (void)cy;
    const unsigned ub = sopaque(WS_UM + (unsigned)((s & 1) * 40 + w * 10 - 2) * 16384u + (((pol >> 2) & 1) << 13));
    f4 ac2[10]; unsigned c0;
    {
      v4u a[10]; v2u a2; f4 acc[3]; v2u sa[3][4], sb[3][4], sc[3][4];
      auto zero = [&]() {
#pragma unroll
        for (int t = 0; t < 3; ++t) { acc[t] = (f4){0, 0, 0, 0}; asm volatile("" : "+v"(acc[t])); } };
      auto mm = [&](auto GC) {
        constexpr int G = decltype(GC)::value, i = G / 3, t = G % 3, j = i < 6 ? i : (i == 6 ? 7 : i == 7 ? 9 : i == 8 ? 6 : 8);
        if constexpr (j & 1) acc[t] = smfmac64(a[j], w1[j >> 1][t], acc[t], sidx);
        else acc[t] = smfmac64(a[j], (j == 0 || j == 6) ? cat(sa[t]) : (j == 4 ? cat(sc[t]) : cat(sb[t])), acc[t], sidx);
        SB();
      };
      const MemGather mg(hb, w);
      SB();
      wait_until(tpass);
      CYC(cy[0]);
      SB();
      mg.load(rw, lane, a[0], a[1], a2);
      c0 = cclk();
#pragma unroll
      for (int i = 0; i < 12; ++i) { sa[i >> 2][i & 3] = rdq(0, i >> 2, i & 3); sb[i >> 2][i & 3] = rdq(1, i >> 2, i & 3); sc[i >> 2][i & 3] = rdq(2, i >> 2, i & 3); }
      zero();
      SB();
      lds16w(mw, a[0]); SB();
      lds16w(mw + 4096, a[1]); SB();
      *(LAS v2u*)mw2 = a2; SB();
      mm(std::integral_constant<int, 0>{});
      first = !MemGather::bad(a[0], a[1], a2);
      mm(std::integral_constant<int, 1>{});
      if (__builtin_expect(!first, 0)) {
        mg.late(rw, lane, a[0], a[1], a2, dead);
        lds16w(mw, a[0]); lds16w(mw + 4096, a[1]); *(LAS v2u*)mw2 = a2;
        zero();
        static_for<2>(mm);
      }
      CYC(cy[1]);
      bar();
      CYC(cy[2]);
      SB();
      static_for<28>([&](auto GC0) {
        constexpr int G = decltype(GC0)::value + 2;
        auto rda = [&](int j) -> v4u { return j < 8 ? lds16(mr[slot_rot(j)] + slot_grp(j) * 4096) : lds16(mr[0] + (j - 8) * 1024); };
        if constexpr (G == 2) { a[2] = rda(2); a[3] = rda(3); a[4] = rda(4); }
        else if constexpr (G < 8) a[G == 3 ? 5 : G == 4 ? 7 : G == 5 ? 9 : G == 6 ? 6 : 8] = rda(G == 3 ? 5 : G == 4 ? 7 : G == 5 ? 9 : G == 6 ? 6 : 8);
        else if constexpr (G < 16) { constexpr int i0 = (3 * (G - 8)) / 2, i1 = (3 * (G - 7)) / 2;
          sa[i0 >> 2][i0 & 3] = rdq(3, i0 >> 2, i0 & 3); if constexpr (i1 - i0 == 2) sa[(i0 + 1) >> 2][(i0 + 1) & 3] = rdq(3, (i0 + 1) >> 2, (i0 + 1) & 3); }
        else if constexpr (G < 24) { constexpr int i0 = (3 * (G - 16)) / 2, i1 = (3 * (G - 15)) / 2;
          sb[i0 >> 2][i0 & 3] = rdq(4, i0 >> 2, i0 & 3); if constexpr (i1 - i0 == 2) sb[(i0 + 1) >> 2][(i0 + 1) & 3] = rdq(4, (i0 + 1) >> 2, (i0 + 1) & 3); }
        SB();
        mm(std::integral_constant<int, G>{});
      });
      CYC(cy[3]);
#pragma unroll
      for (int t = 0; t < 3; ++t) { unsigned d = fold_pack(acc[t]); asm volatile("" : "+v"(d));
        *(LAS unsigned short*)(imw + t * 256) = (unsigned short)d; *(LAS unsigned short*)(imw + t * 256 + 16) = (unsigned short)(d >> 16); SB(); }
#pragma unroll
      for (int t = 0; t < 6; ++t) { ac2[t] = (f4){0, 0, 0, 0}; asm volatile("" : "+v"(ac2[t])); }
    }
    bar();
    CYC(cy[4]);
    {
      const v4u a0 = lds16(imr), a1 = lds16(imr + 1024), a2 = lds16(imr + 2048);
      SB();
#pragma unroll
      for (int t = 6; t < 10; ++t) { ac2[t] = (f4){0, 0, 0, 0}; asm volatile("" : "+v"(ac2[t])); }
      float fa = 0.f, fb = 0.f; unsigned f[12];
      f[0] = f[1] = 0u;
      asm volatile("" : "+v"(f[0]), "+v"(f[1]));
      SB();
      auto sw32 = [&](int i, int j2) { auto r = __builtin_amdgcn_permlane32_swap(f[i], f[j2], false, false); f[i] = r[0]; f[j2] = r[1]; asm volatile("" : "+v"(f[i]), "+v"(f[j2])); };
      auto sw16 = [&](int i, int j2) { auto r = __builtin_amdgcn_permlane16_swap(f[i], f[j2], false, false); f[i] = r[0]; f[j2] = r[1]; asm volatile("" : "+v"(f[i]), "+v"(f[j2])); };
      static_for<41>([&](auto GC) {
        constexpr int G = decltype(GC)::value, t = 2 * (G / 6) + (G & 1), k = (G % 6) >> 1;
        if constexpr (G < 30) ac2[t] = smfmac64(k == 0 ? a0 : k == 1 ? a1 : a2, w2[t < 10 ? t : 0][k], ac2[t], sidx);
        SB();
        if constexpr (G >= 7 && G < 37) {
          constexpr int tp = (G - 7) / 3, ph = (G - 7) % 3;
          if constexpr (ph == 0) { fa = ac2[tp][0] + ac2[tp][2]; asm volatile("" : "+v"(fa)); }
          if constexpr (ph == 1) { fb = ac2[tp][1] + ac2[tp][3]; asm volatile("" : "+v"(fb)); }
          if constexpr (ph == 2) { f[2 + tp] = pk2(fa, fb); asm volatile("" : "+v"(f[2 + tp])); }
        }
        if constexpr (G >= 13 && G % 6 == 1) sw16(2 * ((G - 7) / 6), 2 * ((G - 7) / 6) + 1);
        if constexpr (G >= 14 && G % 12 == 2) sw32(4 * (G / 12 - 1), 4 * (G / 12 - 1) + 2);
        if constexpr (G >= 15 && G % 12 == 3) sw32(4 * (G / 12 - 1) + 1, 4 * (G / 12 - 1) + 3);
        if constexpr (G >= 16 && G % 12 == 4) { constexpr int q = G / 12 - 1;
          st16c(rw, q == 0 ? umo2 : umo, ub + (unsigned)q * 65536u, (v4u){f[4 * q], f[4 * q + 1], f[4 * q + 2], f[4 * q + 3]}); }
        SB();
      });
    }
    if (w == 3 && lane == 0) report(rw, 32 + h, s, cclk() - c0 - 150u, pol >> 3);
    CYC(cy[5]);
    if (w == 3) { STAMP(32 + h, 16 + s * 8 + 3); STAMPV(32 + h, 16 + s * 8 + 7, first ? 0 : 1); }
    if (w == 0 && s + 1 < NSEG) {
      const unsigned e = wait_word(rw, hb + ho_phys(HO_E), dead);
      if (lane == 0) lds4w(V_T, e);
    }
    bar();
    { const unsigned e = __builtin_amdgcn_readfirstlane(lds4(V_T)); tpass = (e >> 8) + (e & 255u); }
  }
}

__device__ __forceinline__ void need_ux(rsrc_t rw, int sc, int& seen, int lane, int w, bool& dead) {
  if (seen < sc) {
    for (unsigned sp = 0; !dead; ++sp) {
      asm volatile("" ::: "memory");
      const int v = (int)ld4c(rw, (w * 8 + (lane & 7)) * 256, WS_CTRL + C_UFLAG * 256);
      if (__all(v >= sc)) { seen = sc; if (__all(v >= sc + 6)) seen = sc + 6; else if (__all(v >= sc + 2)) seen = sc + 2; break; }
      if (sp > SPINCAP) { dead = true; st4c(rw, 0, WS_CTRL + C_TMO * 256, 3500 + sc); }
      __builtin_amdgcn_s_sleep(2);
    }
    __builtin_amdgcn_fence(__ATOMIC_ACQUIRE, "wavefront");
  }
}

constexpr unsigned R_P = 0, R_E = 2048, R_C = 2064;
__device__ __forceinline__ void role_r(const Params& p, unsigned char* ws, rsrc_t rw, int c, int lane, int w) {
  const int g = lane >> 4, n = lane & 15;
  bool dead = false;
  const unsigned pol = stripe_word(rw, lane); unsigned hprev = 0;
  const unsigned p0 = pol >> 3;
  const int sidx = sparse_idx(lane);
  const unsigned l16 = lane * 16u;
  const int fi = 2 * (lane >> 4) + ((lane >> 2) & 1), fq = 2 * (lane & 3) + ((lane >> 3) & 1);
  const unsigned so = g * 256 + n * 8;
  const unsigned prd = opaque(R_P + (fi & 1) * 256 + ((fi >> 1) * 16 + 2 * fq) * 4), pwr = opaque(R_P + w * 512 + lane * 4);
  const unsigned mo = lane * 4;
  const unsigned cadr = opaque(R_C + (lane ? 4 + (lane & 31) * 4 : 0)), cinc = opaque(lane ? 0u : 1u), crd = opaque(R_C);
  unsigned tbulk = rclk();
  int seen = -1;
  if (w == 0 && lane == 0) lds4w(R_C, 0u);
  for (int s = 0; s < NSEG; ++s) {
    const unsigned hb = ho_base(s, pol);
    const unsigned umb = WS_UM + (unsigned)((s & 1) * 40 + c) * 16384u + (((pol >> 2) & 1) << 13);
    const bool lastseg = s + 1 == NSEG;
    need_ux(rw, s, seen, lane, w, dead);
    wait_until(tbulk);
    unsigned cb0 = 0, cb1 = 0, cb2 = 0; (void)cb0; (void)cb1; (void)cb2;
    CYC(cb0);
    v8u ux[4];
#pragma unroll
    for (int k = 0; k < 4; ++k) {
      const unsigned o = ux_at(s, c, w * 8, p0) + 2 * k * 1024;
      const v4u lo = ld16c(rw, l16, o), hi = ld16c(rw, l16, o + 1024);
      ux[k] = (v8u){lo.x, lo.y, lo.z, lo.w, hi.x, hi.y, hi.z, hi.w};
    }
    if (w == 0 && !lastseg) { const unsigned e0 = wait_word(rw, hb + ho_phys(HO_E), dead); if (lane == 0) lds4w(R_E, e0); }
    VMWAIT(0);
    CYC(cb1);
    bar();
    CYC(cb2);
    const unsigned ew = __builtin_amdgcn_readfirstlane(lds4(R_E)), e = ew >> 8;
    unsigned tk1 = 0; (void)tk1;
    const unsigned pbase = lastseg ? hprev : hb;
    const unsigned pm = sopaque(pbase + ho_phys(HO_P + w * 1024)), pxo = sopaque(pbase + ho_phys(HO_P + 4096 + w * 4096));
    const unsigned hn = sopaque(ho_base(s + 1, pol) + ho_phys(HO_MEM + c * 256));
    hprev = hb; tbulk = e + (ew & 255u) + PACE_UM;
    const unsigned umw = sopaque(umb + w * 2048);
    const unsigned ut = sopaque(umt_at(s, c, p0) + w * 2048);
    const unsigned full = sopaque(lastseg ? 0u : 4u * (unsigned)s + 4u);
    v8u u4; unsigned c0, sp = 0, why = 0, cy[8] = {0, 0, 0, 0, 0, 0, 0, 0}; (void)cy;
    {
      v4u pf[5]; f4 d;
      auto mul = [&]() -> bool {
#pragma unroll
        for (int f = 0; f < 4; ++f) { pf[f] = ld16c(rw, l16 + f * 1024, pxo); SB(); }
        const v2u x0 = __builtin_amdgcn_raw_buffer_load_b64(rw, so, pm, 16), x1 = __builtin_amdgcn_raw_buffer_load_b64(rw, so + 128, pm, 16);
        pf[4] = (v4u){x0.x, x0.y, x1.x, x1.y};
        SB();
        const v4u lo = ld16c(rw, l16, umw), hi = ld16c(rw, l16 + 1024, umw);
        u4 = (v8u){lo.x, lo.y, lo.z, lo.w, hi.x, hi.y, hi.z, hi.w};
        SB();
        d = (f4){0, 0, 0, 0};
        asm volatile("" : "+v"(d));
        SB();
#pragma unroll
        for (int f = 0; f < 4; ++f) d = smfmac64(pf[f], ux[f], d, sidx);
        d = smfmac64(pf[4], u4, d, sidx);
        CYC(cy[3]);
        const float s0 = d[0] + d[2], s1 = d[1] + d[3];
        lds4w(pwr, f2u(s0)); lds4w(pwr + 256, f2u(s1));
        SB();
        return __any(__builtin_isunordered(s0, s1));
      };
      if (!lastseg) wait_until(e);
      c0 = cclk();
      CYC(cy[0]);
      SB();
      if (__builtin_expect(mul(), 0)) {
        for (;; ++sp) {
          unsigned m = 0;
#pragma unroll
          for (int f = 0; f < 5; ++f) m |= or4(pf[f]);
          const unsigned m2 = max(max(max(u4[0], u4[1]), max(u4[2], u4[3])), max(max(u4[4], u4[5]), max(u4[6], u4[7])));
          if (sp == 0) why = (__all((m & 0x80008000u) == 0) ? 0 : 1) + (__all(m2 != SENT) ? 0 : 256);
          if (__all((m & 0x80008000u) == 0 && m2 != SENT) || dead) break;
          if (sp > SPINCAP) { dead = true; st4c(rw, 0, WS_CTRL + C_TMO * 256, 3000 + s); }
          asm volatile("" ::: "memory");
          mul();
        }
      }
    }
    CYC(cy[4]);
    {
      unsigned cnt; f2 t0, t1, t2, t3;
      asm volatile("ds_add_u32 %5, %6\n\tds_read_b32 %0, %8\n\t"
                   "ds_read_b64 %1, %7\n\tds_read_b64 %2, %7 offset:512\n\tds_read_b64 %3, %7 offset:1024\n\tds_read_b64 %4, %7 offset:1536\n\t"
                   "s_waitcnt lgkmcnt(0)"
                   : "=&v"(cnt), "=&v"(t0), "=&v"(t1), "=&v"(t2), "=&v"(t3) : "v"(cadr), "v"(cinc), "v"(prd), "v"(crd) : "memory");
      const f2 t = (t0 + t1) + (t2 + t3);
      st4c(rw, cnt == full ? mo : 0xF0000000u, hn, pk2(t[0], t[1]));
      SB();
      CYC(cy[6]);
    }
    bar();
    if (w == 0 && lane == 0) report(rw, 64 + c, s, cclk() - c0, p0);
    (void)why;
    STAMPV(64 + c, 16 + s * 8 + 4 + w, why | ((cy[3] - cy[0]) << 16));
    if (w == 2 && s < 40) { STAMPV(64 + c, 860 + s * 4, cb1 - cb0); STAMPV(64 + c, 861 + s * 4, cb2 - cb0); }
    st16c(rw, l16, ut, (v4u){u4[0], u4[1], u4[2], u4[3]}); st16c(rw, l16, ut + 1024, (v4u){u4[4], u4[5], u4[6], u4[7]});
    st16c(rw, l16, umw, (v4u){SENT, SENT, SENT, SENT}); st16c(rw, l16 + 1024, umw, (v4u){SENT, SENT, SENT, SENT});
    unsigned cb3 = 0, cb4 = 0; (void)cb3; (void)cb4;
    CYC(cb3);
    VMWAIT(0);
    bar();
    CYC(cb4);
    if (w == 2 && s < 40) STAMPV(64 + c, 862 + s * 4, cb4 - cb3);
    if (w == 3 && lane == 0) st4c(rw, 0, WS_CTRL + (C_RFLAG + c) * 256, (unsigned)s);
  }
}

__device__ __forceinline__ void role_t(const Params& p, unsigned char* ws, rsrc_t rw, int t, int lane, int w) {
  const int g = lane >> 4, n = lane & 15;
  bool dead = false;
  rsrc_t rx = mk_rsrc(p.x, S_ * D_ * 2);
  const unsigned l16 = lane * 16u, vm0 = g < 2 ? l16 : 0xF0000000u;
  const int cf = t + 20 * (w >> 1);
  unsigned p0 = 0;
  struct TB { v4u ux[16], um[4], px[20]; v2u xv; };
  TB A, B;
  const unsigned fo = (lane & 15) < 8 ? (C_UFLAG + w * 8 + (lane & 7)) * 256u : (C_RFLAG + t + 20 * (lane & 1)) * 256u;
  int fl = -1;
  auto need = [&](int s) {
    for (unsigned sp = 0; !__all(fl >= s) && !dead; ++sp) {
      asm volatile("" ::: "memory");
      if (sp) __builtin_amdgcn_s_sleep(2);
      fl = (int)ld4c(rw, fo, WS_CTRL);
      if (sp > SPINCAP) { dead = true; st4c(rw, 0, WS_CTRL + C_TMO * 256, 4500 + s); }
    }
    __builtin_amdgcn_fence(__ATOMIC_ACQUIRE, "wavefront");
  };
  auto ld_px = [&](TB& b, int s, int i) {
    b.px[i] = i < 16 ? ld16c(rw, (i & 1) ? l16 : vm0, px_at(s, w * 16 + (i & 8), p0) + (i & 7) * 1024) : ld16c(rw, l16, px_at(s, 64 + w * 4, p0) + (i - 16) * 1024);
  };
  auto ld_unit = [&](TB& b, int s, int k) {
    ld_px(b, s, 2 * k); ld_px(b, s, 2 * k + 1);
#pragma unroll
    for (int q = 0; q < 2; ++q) {
      if (k < 8) b.ux[2 * (k < 8 ? k : 0) + q] = ld16c(rw, l16, ux_at(s, t + 20 * q, w * 8, p0) + k * 1024);
      else b.um[2 * (k < 8 ? 0 : k - 8) + q] = ld16c(rw, l16, umt_at(s, t + 20 * q, p0) + (w * 2 + k - 8) * 1024);
    }
    if (k == 9) b.xv = __builtin_amdgcn_raw_buffer_load_b64(rx, (unsigned)((s * 32 + (w & 1) * 16 + n) * D_ + 16 * cf + 4 * g) * 2, 0, 0);
  };
  const unsigned ho = (unsigned)(((2 * (cf & 1) + (g >> 1)) * 16 + n) * 16 + (g & 1) * 8), hb0 = WS_HF + (unsigned)((w & 1) * 20 + (cf >> 1)) * 1024u;
  for (int s = 0; s < NSEG; ++s) st8c(rw, ho, hb0 + (unsigned)s * 40960u, (v2u){SENT, SENT});
  p0 = stripe_word(rw, lane) >> 3;
  for (int s = 0; s < NSEG; ++s) st16c(rw, l16, px_at(s, t * 4 + w, p0), (v4u){SENT, SENT, SENT, SENT});
  VMWAIT(0);
  bar();
  if (w == 0 && lane == 0) __hip_atomic_fetch_add((unsigned*)(ws + WS_CTRL + C_TARM * 256), 1u, __ATOMIC_RELAXED, __HIP_MEMORY_SCOPE_AGENT);
  need(0);
#pragma unroll
  for (int k = 0; k < 10; ++k) ld_unit(A, 0, k);
  auto seg = [&](int s, TB& cur, TB& nxt) {
    const int sn = s + 1 < NSEG ? s + 1 : s;
    need(sn);
    const unsigned red = opaque((s & 1) * 16384 + l16);
    f4 acc[4];
    auto mul = [&](bool pre) {
#pragma unroll
      for (int i = 0; i < 4; ++i) acc[i] = (f4){0, 0, 0, 0};
#pragma unroll
      for (int k = 0; k < 10; ++k) {
#pragma unroll
        for (int i = 0; i < 4; ++i) acc[i] = mfma32(k < 8 ? cur.ux[k < 8 ? 2 * k + (i >> 1) : 0] : cur.um[k < 8 ? 0 : 2 * (k - 8) + (i >> 1)], cur.px[2 * k + (i & 1)], acc[i]);
        if (pre) { if (k == 0) fl = (int)ld4c(rw, fo, WS_CTRL); ld_unit(nxt, sn, k); }
        SB();
      }
    };
    mul(true);
    for (unsigned sp = 0;; ++sp) {
      const float chk = acc[0][0] + acc[1][0];
      if (!__any(chk != chk)) break;
      unsigned m = 0;
#pragma unroll
      for (int i = 0; i < 20; ++i) m |= or4(cur.px[i]);
      if (__all((m & 0x80008000u) == 0) || dead) break;
      if (sp > SPINCAP) { dead = true; st4c(rw, 0, WS_CTRL + C_TMO * 256, 4000 + s); }
      asm volatile("" ::: "memory");
#pragma unroll
      for (int i = 0; i < 20; ++i) ld_px(cur, s, i);
      mul(false);
    }
#pragma unroll
    for (int i = 0; i < 4; ++i) lds16w(red + (w * 4 + i) * 1024, __builtin_bit_cast(v4u, acc[i]));
    bar();
    const unsigned rr = red + w * 1024;
    const f4 o = (__builtin_bit_cast(f4, lds16(rr)) + __builtin_bit_cast(f4, lds16(rr + 4096))) + (__builtin_bit_cast(f4, lds16(rr + 8192)) + __builtin_bit_cast(f4, lds16(rr + 12288)));
    const v2u xv = cur.xv;
    const v2u hv = {nosent(pk2(bflo(xv.x) + rbf(o[0]), bfhi(xv.x) + rbf(o[1]))), nosent(pk2(bflo(xv.y) + rbf(o[2]), bfhi(xv.y) + rbf(o[3])))};
    st8c(rw, ho, hb0 + (unsigned)s * 40960u, hv);
    if (w == 3 && lane == 0) st4c(rw, 0, WS_CTRL + (C_TFLAG + t) * 256, (unsigned)s);
    if (w == 0) STAMP(104 + t, 16 + s);
  };
  for (int s = 0; s < NSEG; s += 2) { seg(s, A, B); seg(s + 1, B, A); }
}

__device__ __forceinline__ v2u ld8c(rsrc_t r, unsigned v, unsigned s) { return __builtin_amdgcn_raw_buffer_load_b64(r, v, s, 16); }

template <int NS, int NK>
__device__ __forceinline__ void load_wmix(const unsigned short* W, int N, int row0, int sl, int lane, int w, v4u (&wt)[NS][NK]) {
  constexpr int NB = NK / 4;
  const int gg = lane >> 4, hi = lane >> 5, r = (lane >> 2) & 3, cc = lane & 3;
  const int sl0 = min(sl, N / 32 - 2);
  const unsigned short* src = W + (size_t)(row0 + (lane >> 3)) * N + sl0 * 32 + (lane & 7) * 8;
  const unsigned lb = w * 40960;
  auto issue = [&](int blk) {
#pragma unroll
    for (int i = 0; i < 8; ++i) glds16(src + (size_t)(blk * 64 + i * 8) * N, lb + (blk % 5) * 8192 + i * 1024);
  };
#pragma unroll
  for (int b = 0; b < 5; ++b) issue(b);
  const unsigned base = opaque(lb + (8 * hi + r) * 128 + (sl - sl0) * 64 + (gg & 1) * 32 + (2 * (cc & 1) + (cc >> 1)) * 8);
  static_for<NB>([&](auto BC) {
    constexpr int b = decltype(BC)::value, left = NB - 1 - b < 4 ? NB - 1 - b : 4;
    if constexpr (left == 4) VMWAIT(32); else if constexpr (left == 3) VMWAIT(24); else if constexpr (left == 2) VMWAIT(16); else if constexpr (left == 1) VMWAIT(8); else VMWAIT(0);
#pragma unroll
    for (int i = 0; i < NS; ++i)
#pragma unroll
      for (int k = 0; k < 4; ++k) { const unsigned a = base + (b % 5) * 8192 + k * 2048 + i * 64; v2u lo = trread(a), hi2 = trread(a + 512); wt[i][4 * b + k] = (v4u){lo.x, lo.y, hi2.x, hi2.y}; }
    asm volatile("s_waitcnt lgkmcnt(0)" ::: "memory");
    if constexpr (b + 5 < NB) issue(b + 5);
  });
#pragma unroll
  for (int i = 0; i < NS; ++i)
#pragma unroll
    for (int k = 0; k < NK; ++k) if (k * NS < 64) PIN_A(wt[i][k]);
}

constexpr unsigned M_ST = 81920, M_CS = M_ST + 2048;
template <int STAGE, int NS, int NPROD>
__device__ __forceinline__ void role_mix(const Params& p, unsigned char* ws, rsrc_t rw, int j, int lane, int w) {
  constexpr int OKS = STAGE == 3 ? 48 : 20, NG = 40 * NS;
  constexpr unsigned IN = STAGE == 1 ? WS_HF : STAGE == 2 ? WS_H1F : WS_X1F, OUT = STAGE == 1 ? WS_H1F - 20480u : STAGE == 2 ? WS_X1F : WS_H2F;
  constexpr unsigned MYFLAG = STAGE == 1 ? C_S1FLAG : STAGE == 2 ? C_S2FLAG : C_S3FLAG;
  const int g = lane >> 4, n = lane & 15, hi = lane >> 5, tok = lane & 31;
  const unsigned l16 = lane * 16u, OOB = 0xF0000000u;
  bool dead = false;
  const int sl = (j * 4 + w) * NS;
  unsigned vo[NS];
#pragma unroll
  for (int i = 0; i < NS; ++i) {
    const int q = (sl + i) / 5, tt = STAGE == 1 ? tok + 16 + (q == 0 ? 1 : q == 1 ? -1 : q == 2 ? 2 : -2) : tok;
    vo[i] = (unsigned)((tt >> 4) * (OKS * 1024) + (sl + i) * 1024 + (hi * 16 + (tt & 15)) * 16);
  }
  auto put = [&](int s, int c, v4u o) { st16c(rw, vo[c >> 1], OUT + (unsigned)s * (OKS * 2048u) + (c & 1) * 512, o); };
  const bool stat_writer = STAGE == 1 && j == 0 && w == 0;
  const unsigned sto = hi == 0 ? tok * 8u : OOB;
  v4u wt[NS][40];
  load_wmix<NS, 40>(STAGE == 1 ? p.w1 : STAGE == 2 ? p.w2 : p.w3, STAGE == 3 ? F_ : D_, 0, sl, lane, w, wt);
  v4u ones = {0x3F803F80u, 0x3F803F80u, 0x3F803F80u, 0x3F803F80u};
  asm volatile("s_nop 1" : "+v"(ones));
  bar();
  const unsigned csa = opaque(M_CS + w * 512 + hi * 256);
  f4 cu[2] = {{0, 0, 0, 0}, {0, 0, 0, 0}};
  if (STAGE != 2) {
    f16v cs[NS];
    static_for<NG>([&](auto GC) { constexpr int G = decltype(GC)::value, k = G / NS, i = G % NS; mfma_big<k == 0, (k * NS < 64), false>(cs[i], wt[i][k], ones); });
    PAD_BIG();
#pragma unroll
    for (int q = 0; q < 4 * NS; ++q) { const f16v a = cs[q >> 2]; const int o = 4 * (q & 3); lds16w(csa + q * 16, (v4u){f2u(a[o]), f2u(a[o + 1]), f2u(a[o + 2]), f2u(a[o + 3])}); }
    cu[0] = __builtin_bit_cast(f4, lds16(csa)); cu[1] = __builtin_bit_cast(f4, lds16(csa + 16));
  }
  for (int s = 0; s < NSEG; ++s) {
#pragma unroll
    for (int c = 0; c < 2 * NS; ++c) put(s, c, (v4u){SENT, SENT, SENT, SENT});
    if (stat_writer) st8c(rw, sto, WS_ST + (unsigned)s * 256u, (v2u){SENT, SENT});
  }
  if (STAGE == 1) {
#pragma unroll
    for (int c = 0; c < 2 * NS; ++c) { put(-1, c, (v4u){0u, 0u, 0u, 0u}); put(NSEG, c, (v4u){0u, 0u, 0u, 0u}); }
  }
  VMWAIT(0);
  bar();
  Hint hint(STAGE == 1 ? C_TFLAG : STAGE == 2 ? C_S1FLAG : C_S2FLAG, NPROD, lane);
  auto want = [&](int s) { return STAGE == 2 ? min(s + 1, NSEG - 1) : s; };
  f4 s1[2], s2[2];
  auto lda = [&](int s, int q) { return ld16c(rw, l16, IN + (unsigned)((s * 2 + (q & 1)) * 20 + 5 * w + (q >> 1)) * 1024u); };
  auto in_piece = [&](unsigned dst, int q, v4u a) {
    if (q < 2) mfma_v0(s1[q & 1], ones, a); else mfma_v(s1[q & 1], ones, a);
    if (STAGE != 2) { if (q < 2) mfma_v0(s2[q & 1], a, a); else mfma_v(s2[q & 1], a, a); }
    lds16w(dst + q * 1024, a);
    if (q == 9) asm volatile("s_nop 7");
    SB();
  };
  auto in_all = [&](int s, unsigned dst) -> bool {
    v4u a[10]; unsigned m = 0;
#pragma unroll
    for (int q = 0; q < 10; ++q) a[q] = lda(s, q);
#pragma unroll
    for (int q = 0; q < 10; ++q) in_piece(dst, q, a[q]);
    if (__builtin_expect(!__any(__builtin_isunordered(s1[0][0], s1[1][0])), 1)) return true;
#pragma unroll
    for (int q = 0; q < 10; ++q) m = max(m, mx4(a[q]));
    return __all(m != SENT);
  };
  auto in_finish = [&](int s, unsigned dst, bool again) {
    if (again) {
      for (unsigned sp = 0;; ++sp) {
        asm volatile("" ::: "memory");
        if (in_all(s, dst) || dead) break;
        if (sp > SPINCAP) { dead = true; st4c(rw, 0, WS_CTRL + C_TMO * 256, 7100 + s); }
      }
    }
    if (STAGE != 2) {
      const float d0 = (n & 2) ? ((n & 1) ? s2[0][3] : s2[0][2]) : ((n & 1) ? s2[0][1] : s2[0][0]);
      const float d1 = (n & 2) ? ((n & 1) ? s2[1][3] : s2[1][2]) : ((n & 1) ? s2[1][1] : s2[1][0]);
      if (g == (n >> 2)) lds16w(M_ST + (s & 1) * 1024 + w * 256 + n * 16, (v4u){f2u(s1[0][0]), f2u(d0), f2u(s1[1][0]), f2u(d1)});
    }
  };
  const unsigned xo = (g & 1) * 1024 + hi * 256 + n * 16;
  auto xoff = [](int k) { return (k >> 1) * 2048 + (k & 1) * 512; };
  v4u xr[4]; f2 sp4[4];
  auto head = [&](int s) {
    const unsigned src = (s & 1) * 40960 + xo;
    xr[0] = lds16(src); xr[1] = lds16(src + xoff(1)); xr[2] = lds16(src + xoff(2));
    if (STAGE != 2) {
#pragma unroll
      for (int q = 0; q < 4; ++q) sp4[q] = *(LAS f2*)(M_ST + (s & 1) * 1024 + q * 256 + n * 16 + (g & 1) * 8);
    }
    SB();
  };
  auto fetch = [&](int s) {
    hint.need(rw, want(s), dead);
    const unsigned dst = opaque((s & 1) * 40960 + w * 10240 + l16);
    in_finish(s, dst, true);
    bar();
    head(s);
  };
  float pv[16 * NS]; v4u phv[2 * NS]; float prs = 0.f, pnm = 0.f, u[8]; v4u po;
#pragma unroll
  for (int i = 0; i < NS; ++i) { phv[2 * i] = phv[2 * i + 1] = ones; for (int e = 0; e < 16; ++e) pv[16 * i + e] = 0.f; }
  auto epi_step = [&](auto EC, int s) {
    constexpr int E = decltype(EC)::value, c = E / 20, ph = (E % 20) >> 1, e0 = 4 * (E & 1);
    float* y = pv + 8 * c;
#pragma unroll
    for (int e = e0; e < e0 + 4; ++e) {
      if constexpr (STAGE != 2) {
        if constexpr (ph == 0) u[e] = pnm * cu[e >> 2][e & 3];
        if constexpr (ph == 1) y[e] = __builtin_fmaf(y[e], prs, u[e]);
        if constexpr (ph == 2) u[e] = y[e] * y[e];
        if constexpr (ph == 3) u[e] = __builtin_fmaf(u[e], 0.044715f * 2.302208198144325f, 2.302208198144325f);
        if constexpr (ph == 4) u[e] *= y[e];
        if constexpr (ph == 5) u[e] = __builtin_amdgcn_exp2f(u[e]);
        if constexpr (ph == 6) u[e] += 1.f;
        if constexpr (ph == 7) u[e] = __builtin_amdgcn_rcpf(u[e]);
        if constexpr (ph == 8) y[e] = __builtin_fmaf(-y[e], u[e], y[e]);
      } else {
        if constexpr (ph == 0) u[e] = (e & 1) ? bfhi(phv[c][e >> 1]) : bflo(phv[c][e >> 1]);
        if constexpr (ph == 1) u[e] = __builtin_fmaf(u[e], prs, pnm);
        if constexpr (ph == 2) y[e] += u[e];
      }
      if constexpr (ph < 9) asm volatile("" : "+v"(u[e]), "+v"(y[e]));
    }
    if constexpr (STAGE != 2 && ph == 6) cu[E & 1] = __builtin_bit_cast(f4, lds16(csa + ((c + 1) % (2 * NS)) * 32 + (E & 1) * 16));
    if constexpr (ph == 9 && e0 == 0) { po = (v4u){pk2(y[0], y[1]), pk2(y[2], y[3]), pk2(y[4], y[5]), pk2(y[6], y[7])}; asm volatile("" : "+v"(po)); }
    if constexpr (ph == 9 && e0 == 4) {
      put(s, c, (v4u){nosent(po.x), nosent(po.y), nosent(po.z), nosent(po.w)});
      if constexpr (E == NG - 1) {
        if (w == 3 && lane == 0) st4c(rw, 0, WS_CTRL + (MYFLAG + j) * 256, (unsigned)s);
        if (w == 0) STAMP(232 + (STAGE - 1) * 4 + (j & 3), 16 + s);
      }
    }
  };
#pragma unroll
  for (int e = 0; e < 8; ++e) u[e] = 0.f;
  bool pending = false;
  fetch(0);
  for (int s = 0; s < NSEG; ++s) {
    const int sn = s + 1;
    unsigned cy[8] = {0, 0, 0, 0, 0, 0, 0, 0}; (void)cy;
    CYC(cy[0]);
    const bool ready = sn < NSEG && hint.has(want(sn));
    __builtin_amdgcn_fence(__ATOMIC_ACQUIRE, "wavefront");
    const unsigned src = opaque((s & 1) * 40960 + xo), dst = opaque((sn & 1) * 40960 + w * 10240 + l16);
    f16v acc[NS]; v4u hv[2 * NS]; v2u stv = {0, 0}; float mu = 0.f, rstd = 0.f;
    if (STAGE != 2) {
      const f2 st = (sp4[0] + sp4[1]) + (sp4[2] + sp4[3]);
      mu = st[0] * (1.f / D_);
      const float var = fmaxf(st[1] * (1.f / D_) - mu * mu, 0.f);
      rstd = __builtin_amdgcn_rsqf(var + 1e-5f);
      if (stat_writer) st8c(rw, sto, WS_ST + (unsigned)s * 256u, (v2u){nosent(f2u(mu)), nosent(f2u(rstd))});
      if (STAGE == 1) rstd *= __builtin_amdgcn_rsqf(var * rstd * rstd + 1e-5f);
    }
    SB();
    auto body = [&](auto PRE, auto EPI) {
      constexpr int AB = NS == 1 ? 10 : 5, AP = 4 * AB;
      v4u an[AB];
      static_for<NG>([&](auto GC) {
        constexpr int G = decltype(GC)::value, k = G / NS, i = G % NS;
        mfma_big<k == 0, (k * NS < 64), false>(acc[i], wt[i][k], xr[k & 3]);
        SB();
        if constexpr (i == 0 && k < 37) xr[(k + 3) & 3] = lds16(src + xoff(k + 3));
        if constexpr (i == NS - 1) {
          if constexpr (decltype(PRE)::value && k % AP < AB) an[k % AP] = lda(sn, (k / AP) * AB + k % AP);
          if constexpr (k == 10) hint.sample(rw);
          if constexpr (STAGE == 2 && k == 11) stv = ld8c(rw, tok * 8u, WS_ST + (unsigned)s * 256u);
          if constexpr (STAGE == 2 && k >= 12 && k < 12 + 2 * NS) hv[k - 12] = ld16c(rw, vo[(k - 12) >> 1], WS_HF + (unsigned)s * 40960u + ((k - 12) & 1) * 512);
        }
        if constexpr (decltype(EPI)::value) epi_step(GC, s - 1);
        SB();
        if constexpr (decltype(PRE)::value && i == NS - 1 && k % AP >= AP - AB) in_piece(dst, (k / AP) * AB + k % AP - (AP - AB), an[k % AP - (AP - AB)]);
      });
      PAD_BIG();
      if (STAGE == 2) {
        for (unsigned sp = 0; __any(max(stv.x, stv.y) == SENT) && !dead; ++sp) {
          asm volatile("" ::: "memory");
          stv = ld8c(rw, tok * 8u, WS_ST + (unsigned)s * 256u);
          if (sp > SPINCAP) { dead = true; st4c(rw, 0, WS_CTRL + C_TMO * 256, 7200 + s); }
        }
        mu = u2f(stv.x); rstd = u2f(stv.y);
#pragma unroll
        for (int c = 0; c < 2 * NS; ++c) phv[c] = hv[c];
      }
#pragma unroll
      for (int e = 0; e < 16 * NS; ++e) pv[e] = acc[e >> 4][e & 15];
      prs = rstd; pnm = -mu * rstd;
    };
    if (ready) {
      CYC(cy[2]);
      if (pending) body(std::true_type{}, std::true_type{}); else body(std::true_type{}, std::false_type{});
      CYC(cy[3]);
      in_finish(sn, dst, __builtin_expect(__any(__builtin_isunordered(s1[0][0], s1[1][0])), 0));
      bar();
      head(sn);
      CYC(cy[4]);
      pending = true;
    } else {
      if (pending) body(std::false_type{}, std::true_type{}); else body(std::false_type{}, std::false_type{});
      static_for<NG>([&](auto EC) { epi_step(EC, s); SB(); });
      pending = false;
      if (sn == NSEG) break;
      fetch(sn);
    }
  }
}

template <int NS3>
__device__ __forceinline__ void role_mix4(const Params& p, unsigned char* ws, rsrc_t rw, int j, int lane, int w) {
  const int g = lane >> 4, n = lane & 15, hi = lane >> 5;
  const unsigned l16 = lane * 16u;
  bool dead = false;
  v4u wt[2][24];
  load_wmix<2, 24>(p.w4, D_, 384 * w, 2 * j, lane, w, wt);
  bar();
  Hint hint(C_S3FLAG, NS3, lane);
  const int slab = 2 * j + (w >> 1);
  const unsigned ao = (g & 1) * 49152u + hi * 256 + n * 16, fo = (g & 1) * 20480u + slab * 1024 + (w & 1) * 512 + hi * 256 + n * 16;
  v4u a[24];
  auto lda = [&](int s, int k) { a[k] = ld16c(rw, ao, WS_H2F + (unsigned)(s * 96 + 12 * w + (k >> 1)) * 1024u + (k & 1) * 512); };
  hint.need(rw, 0, dead);
#pragma unroll
  for (int q = 0; q < 24; ++q) lda(0, q);
  for (int s = 0; s < NSEG; ++s) {
    const int sn = s + 1 < NSEG ? s + 1 : s;
    hint.need(rw, sn, dead);
    const v4u hv = ld16c(rw, fo, WS_HF + (unsigned)s * 40960u), xv = ld16c(rw, fo, WS_X1F + (unsigned)s * 40960u);
    f16v acc[2];
    auto body = [&]() {
      static_for<24>([&](auto KC) {
        constexpr int k = decltype(KC)::value;
        mfma_big<k == 0, true, false>(acc[0], wt[0][k], a[k]);
        if constexpr (k == 8) hint.sample(rw);
        SB();
        mfma_big<k == 0, true, false>(acc[1], wt[1][k], a[k]);
        SB();
        lda(sn, k);
        SB();
      });
      PAD_BIG();
    };
    body();
    if (__builtin_expect(__any(acc[0][0] != acc[0][0]), 0)) {
      for (unsigned sp = 0;; ++sp) {
        asm volatile("" ::: "memory");
        unsigned m = 0;
#pragma unroll
        for (int q = 0; q < 24; ++q) lda(s, q);
#pragma unroll
        for (int q = 0; q < 24; ++q) m = max(m, mx4(a[q]));
        if (__all(m != SENT) || dead) break;
        if (sp > SPINCAP) { dead = true; st4c(rw, 0, WS_CTRL + C_TMO * 256, 7300 + s); }
      }
      body();
    }
    const unsigned red = opaque((s & 1) * 32768 + l16);
#pragma unroll
    for (int i = 0; i < 8; ++i) { const f16v d = acc[i >> 2]; const int o = 4 * (i & 3);
      lds16w(red + (w * 8 + i) * 1024, (v4u){f2u(d[o]), f2u(d[o + 1]), f2u(d[o + 2]), f2u(d[o + 3])}); }
    bar();
    f4 o[2];
#pragma unroll
    for (int tt = 0; tt < 2; ++tt) {
      const unsigned rr = red + (w * 2 + tt) * 1024;
      o[tt] = (__builtin_bit_cast(f4, lds16(rr)) + __builtin_bit_cast(f4, lds16(rr + 8192))) + (__builtin_bit_cast(f4, lds16(rr + 16384)) + __builtin_bit_cast(f4, lds16(rr + 24576)));
    }
    v4u r;
#pragma unroll
    for (int q = 0; q < 4; ++q)
      r[q] = pk2(bflo(hv[q]) + rbf(bflo(xv[q]) + rbf(o[q >> 1][2 * (q & 1)])), bfhi(hv[q]) + rbf(bfhi(xv[q]) + rbf(o[q >> 1][2 * (q & 1) + 1])));
    *(v4u*)(p.out + (size_t)(s * 32 + (lane & 31)) * D_ + slab * 32 + (w & 1) * 16 + hi * 8) = r;
    if (w == 0) STAMP(244 + (j & 3), 16 + s);
  }
}

__global__ void __launch_bounds__(256, 1) k_main(Params p) {
  extern __shared__ __attribute__((aligned(16))) unsigned char lds_raw[];
  unsigned char* ws = ws_base(p.ws);
  rsrc_t rw = mk_rsrc(ws, WS_END);
  const int tid = threadIdx.x, w = __builtin_amdgcn_readfirstlane(tid >> 6);
  if (tid == 0) {
    const unsigned xcc = (unsigned)__builtin_amdgcn_s_getreg(20 | (3 << 11)) & 7u;
    unsigned v = 0;
    for (unsigned k = 0; k < 8; ++k) {
      const unsigned x = (xcc + k) & 7u;
      const unsigned rk = __hip_atomic_fetch_add((unsigned*)(ws + WS_CTRL + (C_TICKET + x) * 256), 1u, __ATOMIC_RELAXED, __HIP_MEMORY_SCOPE_AGENT) + 1u;
      if (rk < 32u) { v = rk * 8 + x; break; }
    }
    lds4w(0, v);
  }
  bar();
  const int v = __builtin_amdgcn_readfirstlane(lds4(0));
  bar();
  const int x = v & 7, idx = (v >> 3) * 4 + (x & 3);
  if (x < 4) {
    if (idx < 32) role_y(p, ws, rw, idx, lane_id(), w);
    else if (idx < 64) role_v(p, ws, rw, idx - 32, lane_id(), w);
    else if (idx < 104) role_r(p, ws, rw, idx - 64, lane_id(), w);
    else if (idx < 124) role_t(p, ws, rw, idx - 104, lane_id(), w);
    else if (idx == 124) {
      if (w > 0) return;
      const int lane = lane_id();
      const unsigned pol = stripe_word(rw, lane);
      const unsigned t00 = rclk(), c00 = cclk();
      unsigned t = t00, esty = 3000, estr = 1450, c1 = 140, c3 = 72; bool dead = false;
      for (int s = 0; s + 1 < NSEG; ++s) {
        const unsigned hb = ho_base(s, pol);
        wait_until(t);
        unsigned e = t + c1;
        for (unsigned sp = 0;; ++sp) {
          asm volatile("" ::: "memory");
          unsigned m = 0;
#pragma unroll
          for (int k = 0; k < 10; ++k) m = max(m, mx4(ld16c(rw, lane * 16, hb + ho_phys(HO_MEM + k * 1024))));
          if (__all(m != SENT) || dead) { if (sp) e = rclk() + c1 - PACE_LATE; break; }
          if (sp > SPINCAP) { dead = true; st4c(rw, 0, WS_CTRL + C_TMO * 256, 6000 + s); }
        }
        if (lane == 0) st4c(rw, 0, hb + ho_phys(HO_E), (e << 8) | c3);
        STAMPV(124, 32 + s, c1 * 256 + c3);
        t = e + c3;
        if (s >= 2) {
          unsigned a = ld4c(rw, lane * 128, stripe(WS_REP, 0, pol >> 3)), b = ld4c(rw, lane * 128, stripe(WS_REP, 8192, pol >> 3));
          a = (a >> 16) == (unsigned)(s - 1) ? a & 65535u : 0u; b = ((b >> 16) == (unsigned)(s - 1) && lane < 40) ? b & 65535u : 0u;
#pragma unroll
          for (int o = 1; o < 64; o <<= 1) { a = max(a, (unsigned)__shfl_xor((int)a, o)); b = max(b, (unsigned)__shfl_xor((int)b, o)); }
          a = __builtin_amdgcn_readfirstlane(a); b = __builtin_amdgcn_readfirstlane(b);
          const unsigned dn = s < 10 ? 40u : 6u;
          if (a > 1000u && a < esty + 400u) esty = a > esty ? min(a, esty + 30u) : max(a, esty - dn);
          if (b > 500u && b < estr + 400u) estr = b > estr ? min(b, estr + 30u) : max(b, estr - dn);
          const float tpc = (float)(rclk() - t00) * __builtin_amdgcn_rcpf((float)(cclk() - c00));
          c1 = __builtin_amdgcn_readfirstlane((unsigned)((float)esty * tpc)) + PACE_C1;
          c3 = min(__builtin_amdgcn_readfirstlane((unsigned)((float)estr * tpc)) + PACE_C3, 250u);
        }
      }
    }
    else role_mix<1, 1, 20>(p, ws, rw, idx - 125, lane_id(), w);
  } else {
    if (idx < 72) role_wqk(p, ws, rw, idx, lane_id(), w);
    else if (idx < 104) role_v2(p, ws, rw, idx - 72, lane_id(), w);
    else if (idx < 114) role_mix4<6>(p, ws, rw, idx - 104, lane_id(), w);
    else if (idx < 120) role_mix<3, 2, 5>(p, ws, rw, idx - 114, lane_id(), w);
    else if (idx < 125) role_mix<2, 1, 5>(p, ws, rw, idx - 120, lane_id(), w);
    else if (idx < 127) role_mix<1, 1, 20>(p, ws, rw, idx - 122, lane_id(), w);
  }
}

extern "C" void kernel_launch(void* const* d_in, const int* in_sizes, int n_in, void* d_out, int out_size, void* d_ws, size_t ws_size,
                              hipStream_t stream) {
  Params p;
  p.x = (const unsigned short*)d_in[0]; p.mem0 = (const unsigned short*)d_in[1];
  p.wq = (const unsigned short*)d_in[2]; p.wk = (const unsigned short*)d_in[3]; p.wv = (const unsigned short*)d_in[4]; p.wo = (const unsigned short*)d_in[5];
  p.w1 = (const unsigned short*)d_in[8]; p.w2 = (const unsigned short*)d_in[9]; p.w3 = (const unsigned short*)d_in[10]; p.w4 = (const unsigned short*)d_in[11];
  p.out = (unsigned short*)d_out; p.ws = (unsigned char*)d_ws;
  (void)hipFuncSetAttribute((const void*)k_main, hipFuncAttributeMaxDynamicSharedMemorySize, LDS_BYTES);
  k_init<<<INIT_CAL + INIT_LN + INIT_FILL, 256, 0, stream>>>(p);
  k_main<<<256, 256, LDS_BYTES, stream>>>(p);
}
```

```cpp
#include <hip/hip_runtime.h>
#include <hip/hip_bf16.h>
#include <type_traits>
#include <utility>

constexpr int S_ = 2048, D_ = 640, H_ = 32, E_ = 192, HE_ = H_ * E_, NSEG = 64, F_ = 1536;

#define LAS __attribute__((address_space(3)))
typedef unsigned v4u __attribute__((ext_vector_type(4)));
typedef unsigned v2u __attribute__((ext_vector_type(2)));
typedef unsigned v8u __attribute__((ext_vector_type(8)));
typedef float f4 __attribute__((ext_vector_type(4)));
typedef float f2 __attribute__((ext_vector_type(2)));
typedef float f16v __attribute__((ext_vector_type(16)));
typedef __bf16 bf2v __attribute__((ext_vector_type(2)));
typedef __bf16 bf8v __attribute__((ext_vector_type(8)));
typedef __bf16 bf16v __attribute__((ext_vector_type(16)));
typedef short s4v __attribute__((ext_vector_type(4)));
typedef __amdgpu_buffer_rsrc_t rsrc_t;

constexpr unsigned MB = 1u << 20;
constexpr unsigned WS_HO = 0;
constexpr unsigned WS_UM = 4 * MB;
constexpr unsigned WS_CTRL = 6 * MB;
constexpr unsigned WS_CAL = 6 * MB + 128 * 1024;
constexpr unsigned WS_REP = 6 * MB + 256 * 1024;
constexpr unsigned WS_PX = 8 * MB;
constexpr unsigned WS_XF = 18 * MB;
constexpr unsigned WS_HF = 21 * MB;
constexpr unsigned WS_H1F = 24 * MB;
constexpr unsigned WS_X1F = 27 * MB;
constexpr unsigned WS_H2F = 30 * MB;
constexpr unsigned WS_ST = 37 * MB;
constexpr unsigned WS_XST = 37 * MB + 65536;
constexpr unsigned WS_QKV = 38 * MB;
constexpr unsigned WS_UX = 86 * MB;
constexpr unsigned WS_UMT = 166 * MB;
#define SPL 48
constexpr int S3_SPLIT = SPL;
#define WEAG 4
#define VEAG 1
#define LEAD0 16
#define LEADS 32
#define RNG 16
constexpr int RING = RNG, W_EAGER = WEAG, V_EAGER = VEAG, NSETUP = 88, ARM = 3;
constexpr unsigned WS_END = 212 * MB;

constexpr unsigned WS_DBG = 211 * MB;
constexpr unsigned HO_P = 0, HO_MEM = 20480, HO_E = 30720;
#define PACE_C1 16
#define PACE_C3 10
#define PACE_UM 25
#define PACE_LATE 28
constexpr unsigned C_TICKET = 0, C_TMO = 9, C_SET1 = 10, C_WFLAG = 16, C_UFLAG = 96, C_TFLAG = 168, C_S1FLAG = 192, C_S2FLAG = 200, C_S3FLAG = 208, C_HFLAG = 216;
constexpr unsigned SENT = 0xFFFFFFFFu;
constexpr unsigned SPINCAP = 1u << 18;
constexpr int LDS_BYTES = 160 * 1024;

struct Params {
  const unsigned short* x; const unsigned short* mem0;
  const unsigned short* wq; const unsigned short* wk; const unsigned short* wv; const unsigned short* wo;
  const unsigned short* w1; const unsigned short* w2; const unsigned short* w3; const unsigned short* w4;
  unsigned short* out; unsigned char* ws;
};

__device__ __forceinline__ unsigned char* ws_base(unsigned char* ws) {
  return ws + ((0u - (unsigned)(size_t)ws) & (2 * MB - 1));
}
__device__ __forceinline__ rsrc_t mk_rsrc(const void* p, unsigned bytes) {
  return __builtin_amdgcn_make_buffer_rsrc((void*)p, 0, bytes, 0x00020000);
}
__device__ __forceinline__ v4u ld16c(rsrc_t r, unsigned v, unsigned s) { return __builtin_amdgcn_raw_buffer_load_b128(r, v, s, 16); }
__device__ __forceinline__ unsigned ld4c(rsrc_t r, unsigned v, unsigned s) { return __builtin_amdgcn_raw_buffer_load_b32(r, v, s, 16); }
__device__ __forceinline__ void st16c(rsrc_t r, unsigned v, unsigned s, v4u d) { __builtin_amdgcn_raw_buffer_store_b128(d, r, v, s, 16); }
__device__ __forceinline__ void st8c(rsrc_t r, unsigned v, unsigned s, v2u d) { __builtin_amdgcn_raw_buffer_store_b64(d, r, v, s, 16); }
__device__ __forceinline__ void st4c(rsrc_t r, unsigned v, unsigned s, unsigned d) { __builtin_amdgcn_raw_buffer_store_b32(d, r, v, s, 16); }
__device__ __forceinline__ void st2c(rsrc_t r, unsigned v, unsigned s, unsigned short d) { __builtin_amdgcn_raw_buffer_store_b16(d, r, v, s, 16); }
__device__ __forceinline__ v4u ld16p(rsrc_t r, unsigned v, unsigned s) { return __builtin_amdgcn_raw_buffer_load_b128(r, v, s, 0); }
__device__ __forceinline__ unsigned ld4p(rsrc_t r, unsigned v, unsigned s) { return __builtin_amdgcn_raw_buffer_load_b32(r, v, s, 0); }

__device__ __forceinline__ unsigned f2u(float a) { return __builtin_bit_cast(unsigned, a); }
__device__ __forceinline__ float u2f(unsigned a) { return __builtin_bit_cast(float, a); }
__device__ __forceinline__ unsigned pk2(float a, float b) { bf2v t = {(__bf16)a, (__bf16)b}; return __builtin_bit_cast(unsigned, t); }
__device__ __forceinline__ unsigned pk2a(float a, float b) { unsigned r; asm("v_cvt_pk_bf16_f32 %0, |%1|, |%2|" : "=v"(r) : "v"(a), "v"(b)); return r; }
__device__ __forceinline__ float bflo(unsigned u) { return __builtin_bit_cast(float, u << 16); }
__device__ __forceinline__ float bfhi(unsigned u) { return __builtin_bit_cast(float, u & 0xFFFF0000u); }
__device__ __forceinline__ float rbf(float a) { return (float)(__bf16)a; }
__device__ __forceinline__ v4u pk8(f4 a, f4 b) { return (v4u){pk2(a[0], a[1]), pk2(a[2], a[3]), pk2(b[0], b[1]), pk2(b[2], b[3])}; }

__device__ __forceinline__ f4 mfma32(v4u a, v4u b, f4 c) {
  return __builtin_amdgcn_mfma_f32_16x16x32_bf16(__builtin_bit_cast(bf8v, a), __builtin_bit_cast(bf8v, b), c, 0, 0, 0);
}
__device__ __forceinline__ f4 smfmac64(v4u a, v8u b, f4 c, int idx) {
  return __builtin_amdgcn_smfmac_f32_16x16x64_bf16(__builtin_bit_cast(bf8v, a), __builtin_bit_cast(bf16v, b), c, idx, 0, 0);
}
template <bool FIRST, bool WA, bool WB> __device__ __forceinline__ void mfma_big(f16v& d, v4u a, v4u b) {
  if (FIRST) {
    if (WA) asm volatile("v_mfma_f32_32x32x16_bf16 %0, %1, %2, 0" : "=&v"(d) : "a"(a), "v"(b));
    else if (WB) asm volatile("v_mfma_f32_32x32x16_bf16 %0, %1, %2, 0" : "=&v"(d) : "v"(a), "a"(b));
    else asm volatile("v_mfma_f32_32x32x16_bf16 %0, %1, %2, 0" : "=&v"(d) : "v"(a), "v"(b));
  } else {
    if (WA) asm volatile("v_mfma_f32_32x32x16_bf16 %0, %1, %2, %0" : "+v"(d) : "a"(a), "v"(b));
    else if (WB) asm volatile("v_mfma_f32_32x32x16_bf16 %0, %1, %2, %0" : "+v"(d) : "v"(a), "a"(b));
    else asm volatile("v_mfma_f32_32x32x16_bf16 %0, %1, %2, %0" : "+v"(d) : "v"(a), "v"(b));
  }
}
#define PAD_BIG() do { __builtin_amdgcn_sched_barrier(0); asm volatile("s_nop 7\n\ts_nop 3"); __builtin_amdgcn_sched_barrier(0); } while (0)
__device__ __forceinline__ void mfma_v0(f4& d, v4u a, v4u b) { asm volatile("s_nop 1\n\tv_mfma_f32_16x16x32_bf16 %0, %1, %2, 0" : "=&v"(d) : "v"(a), "v"(b)); }
__device__ __forceinline__ void mfma_v(f4& d, v4u a, v4u b) { asm volatile("s_nop 1\n\tv_mfma_f32_16x16x32_bf16 %0, %1, %2, %0" : "+v"(d) : "v"(a), "v"(b)); }
__device__ __forceinline__ float vmax2(float a, float b) { float r; asm("v_max_f32 %0, %1, %2" : "=v"(r) : "v"(a), "v"(b)); return r; }
__device__ __forceinline__ float vmax3(float a, float b, float c) { float r; asm("v_max3_f32 %0, %1, %2, %3" : "=v"(r) : "v"(a), "v"(b), "v"(c)); return r; }
__device__ __forceinline__ v2u trread(unsigned lds_addr) {
  return __builtin_bit_cast(v2u, __builtin_amdgcn_ds_read_tr16_b64_v4i16((LAS s4v*)lds_addr));
}
__device__ __forceinline__ v4u lds16(unsigned a) { return *(LAS v4u*)a; }
__device__ __forceinline__ void lds16w(unsigned a, v4u v) { *(LAS v4u*)a = v; }
__device__ __forceinline__ void lds4w(unsigned a, unsigned v) { *(LAS unsigned*)a = v; }
__device__ __forceinline__ unsigned lds4(unsigned a) { return *(LAS unsigned*)a; }

__device__ __forceinline__ void glds16(const void* gsrc, unsigned dst) {
  unsigned keep;
  asm volatile("s_mov_b32 %0, m0\n\ts_mov_b32 m0, %2\n\ts_nop 0\n\tglobal_load_lds_dwordx4 %1, off\n\ts_mov_b32 m0, %0"
               : "=&s"(keep) : "v"(gsrc), "s"(dst) : "memory");
}
#define VMWAIT(n) asm volatile("s_waitcnt vmcnt(" #n ")" ::: "memory")
__device__ __forceinline__ void bar() {
  asm volatile("s_waitcnt lgkmcnt(0)" ::: "memory");
  __builtin_amdgcn_s_barrier();
  asm volatile("" ::: "memory");
}
__device__ __forceinline__ unsigned nosent(unsigned v) { return v < 0xFFFFFFFEu ? v : 0xFFFFFFFEu; }
__device__ __forceinline__ unsigned mx4(v4u v) { return max(max(v.x, v.y), max(v.z, v.w)); }
__device__ __forceinline__ unsigned or4(v4u v) { return (v.x | v.y) | (v.z | v.w); }
__device__ __forceinline__ unsigned ho_phys(unsigned o) { return ((o >> 13) << 14) | (o & 8191u); }

__device__ __forceinline__ float xor16_max(float v) { auto r = __builtin_amdgcn_permlane16_swap(f2u(v), f2u(v), false, false);
  return vmax2(u2f(r[0]), u2f(r[1])); }
__device__ __forceinline__ float xor32_max(float v) { auto r = __builtin_amdgcn_permlane32_swap(f2u(v), f2u(v), false, false);
  return vmax2(u2f(r[0]), u2f(r[1])); }
__device__ __forceinline__ float xor16_add(float v) { auto r = __builtin_amdgcn_permlane16_swap(f2u(v), f2u(v), false, false);
  return u2f(r[0]) + u2f(r[1]); }
__device__ __forceinline__ float xor32_add(float v) { auto r = __builtin_amdgcn_permlane32_swap(f2u(v), f2u(v), false, false);
  return u2f(r[0]) + u2f(r[1]); }
__device__ __forceinline__ float ror8(float v) {
  return __builtin_bit_cast(float, __builtin_amdgcn_update_dpp(0, __builtin_bit_cast(int, v), 0x128, 0xf, 0xf, true));
}
__device__ __forceinline__ void row16_max(float& a, float& b) {
  asm volatile("s_nop 1\n\t"
               "v_max_f32_dpp %0, %0, %0 row_ror:8 row_mask:0xf bank_mask:0xf\n\tv_max_f32_dpp %1, %1, %1 row_ror:8 row_mask:0xf bank_mask:0xf\n\ts_nop 0\n\t"
               "v_max_f32_dpp %0, %0, %0 row_ror:4 row_mask:0xf bank_mask:0xf\n\tv_max_f32_dpp %1, %1, %1 row_ror:4 row_mask:0xf bank_mask:0xf\n\ts_nop 0\n\t"
               "v_max_f32_dpp %0, %0, %0 row_ror:2 row_mask:0xf bank_mask:0xf\n\tv_max_f32_dpp %1, %1, %1 row_ror:2 row_mask:0xf bank_mask:0xf\n\ts_nop 0\n\t"
               "v_max_f32_dpp %0, %0, %0 row_ror:1 row_mask:0xf bank_mask:0xf\n\tv_max_f32_dpp %1, %1, %1 row_ror:1 row_mask:0xf bank_mask:0xf"
               : "+v"(a), "+v"(b));
}
__device__ __forceinline__ void row16_add(float& a, float& b) {
  asm volatile("s_nop 1\n\t"
               "v_add_f32_dpp %0, %0, %0 row_ror:8 row_mask:0xf bank_mask:0xf\n\tv_add_f32_dpp %1, %1, %1 row_ror:8 row_mask:0xf bank_mask:0xf\n\ts_nop 0\n\t"
               "v_add_f32_dpp %0, %0, %0 row_ror:4 row_mask:0xf bank_mask:0xf\n\tv_add_f32_dpp %1, %1, %1 row_ror:4 row_mask:0xf bank_mask:0xf\n\ts_nop 0\n\t"
               "v_add_f32_dpp %0, %0, %0 row_ror:2 row_mask:0xf bank_mask:0xf\n\tv_add_f32_dpp %1, %1, %1 row_ror:2 row_mask:0xf bank_mask:0xf\n\ts_nop 0\n\t"
               "v_add_f32_dpp %0, %0, %0 row_ror:1 row_mask:0xf bank_mask:0xf\n\tv_add_f32_dpp %1, %1, %1 row_ror:1 row_mask:0xf bank_mask:0xf"
               : "+v"(a), "+v"(b));
}
__device__ __forceinline__ unsigned fold_pack(f4 a) { return pk2(a[0] + a[2], a[1] + a[3]); }
__device__ __forceinline__ int sparse_idx(int lane) { return (lane & 2) ? 0xEEEE : 0x4444; }
__device__ __forceinline__ unsigned cclk() { return (unsigned)__builtin_amdgcn_s_memtime(); }
__device__ __forceinline__ unsigned rclk() { return (unsigned)__builtin_amdgcn_s_memrealtime(); }
__device__ __forceinline__ void wait_until(unsigned t) {
  unsigned d, cnt = 8192;
  asm volatile("1:\n\t"
               "s_memrealtime vcc\n\t"
               "s_sub_u32 %1, %1, 1\n\t"
               "s_cbranch_scc1 3f\n\t"
               "s_waitcnt lgkmcnt(0)\n\t"
               "s_sub_u32 %0, vcc_lo, %2\n\t"
               "s_bitcmp1_b32 %0, 23\n\t"
               "s_cbranch_scc1 1b\n\t"
               "s_and_b32 %0, %0, 0xffffff\n\t"
               "s_cmp_ge_u32 %0, 3\n\ts_cbranch_scc1 3f\n\ts_nop 3\n\t"
               "s_cmp_ge_u32 %0, 2\n\ts_cbranch_scc1 3f\n\ts_nop 3\n\t"
               "s_cmp_ge_u32 %0, 1\n\ts_cbranch_scc1 3f\n\ts_nop 10\n\t"
               "3:\n\t"
               "s_waitcnt lgkmcnt(0)"
               : "=&s"(d), "+s"(cnt) : "s"(__builtin_amdgcn_readfirstlane(t - 4u)) : "scc", "vcc", "memory");
}
__device__ __forceinline__ int lane_id() { int l; asm volatile("v_mbcnt_lo_u32_b32 %0, -1, 0\n\tv_mbcnt_hi_u32_b32 %0, -1, %0" : "=v"(l)); return l & 63; }
__device__ __forceinline__ unsigned sopaque(unsigned v) { asm volatile("" : "+s"(v)); return v; }
#define STAMP(id, slot) do {} while (0)
#define STAMPV(id, slot, v) do {} while (0)
#define MARK(id, s) do {} while (0)
__device__ __forceinline__ unsigned opaque(unsigned v) { asm volatile("" : "+v"(v)); return v; }
template <class F, int... I> __device__ __forceinline__ void static_for_impl(F&& f, std::integer_sequence<int, I...>) { (f(std::integral_constant<int, I>{}), ...); }
template <int N, class F> __device__ __forceinline__ void static_for(F&& f) { static_for_impl(f, std::make_integer_sequence<int, N>{}); }
#define CYC(x) do {} while (0)
#define SB() __builtin_amdgcn_sched_barrier(0)
#define PIN_A(x) asm volatile("" : "+a"(x))
__device__ __forceinline__ unsigned wait_word(rsrc_t rw, unsigned off, bool& dead) {
  unsigned v = 0;
  for (unsigned sp = 0; !dead; ++sp) {
    asm volatile("" ::: "memory");
    v = __builtin_amdgcn_readfirstlane(ld4c(rw, 0, off));
    if (v != SENT) break;
    if (sp > SPINCAP) { dead = true; st4c(rw, 0, WS_CTRL + C_TMO * 256, 5000); }
  }
  return v;
}
__device__ __forceinline__ unsigned stripe_word(rsrc_t rw, int lane) {
  int v = (int)ld4p(rw, (lane & 15) * 4, WS_CAL);
  v += __shfl_xor(v, 1); v += __shfl_xor(v, 2); v += __shfl_xor(v, 4); v += __shfl_xor(v, 8);
  return __builtin_amdgcn_readfirstlane(v) > 0 ? 13u : 2u;
}
__device__ __forceinline__ unsigned stripe(unsigned base, unsigned o, unsigned p0) {
  const unsigned t = base + ((o >> 13) << 14) + (o & 8191u);
  return t | (((p0 ^ (t >> 21)) & 1u) << 13);
}
#define HOME_QK 0
#define HOME_UX 0
__device__ __forceinline__ unsigned qk_at(int s, int h, int frag, unsigned p0) { const unsigned o = (unsigned)(((s & (RING - 1)) * 32 + h) * 24 + frag) * 1024u;
  return stripe(WS_QKV, o, HOME_QK == 2 ? p0 ^ (o >> 13) : p0 ^ HOME_QK); }
__device__ __forceinline__ unsigned ux_at(int s, int c, int h, unsigned p0) { const unsigned o = (unsigned)(((s & (RING - 1)) * 40 + c) * 32 + h) * 1024u;
  return stripe(WS_UX, o, HOME_UX == 2 ? p0 ^ (o >> 13) : p0 ^ HOME_UX); }
__device__ __forceinline__ unsigned px_at(int s, int frag, unsigned p0) { return stripe(WS_PX, (unsigned)((s & (RING - 1)) * 80 + frag) * 1024u, p0); }
__device__ __forceinline__ unsigned umt_at(int s, int c, unsigned p0) { return stripe(WS_UMT, (unsigned)((s & (RING - 1)) * 40 + c) * 8192u, p0); }
__device__ __forceinline__ void report(rsrc_t rw, int id, int s, unsigned cyc, unsigned p0) { st4c(rw, 0, stripe(WS_REP, id * 128u, p0), ((unsigned)s << 16) | min(cyc, 65535u)); }
__device__ __forceinline__ unsigned ho_base(int s, unsigned pol) { return WS_HO + (unsigned)s * 65536u + (((pol >> (s >> 5)) & 1u) << 13); }
__device__ __forceinline__ int wait_ge(rsrc_t rw, unsigned word, int want, bool& dead) {
  int v = -1;
  for (unsigned sp = 0; !dead; ++sp) {
    asm volatile("" ::: "memory");
    v = (int)__builtin_amdgcn_readfirstlane(ld4c(rw, 0, WS_CTRL + word * 256));
    if (v >= want) break;
    if (sp > SPINCAP) { dead = true; st4c(rw, 0, WS_CTRL + C_TMO * 256, word); }
    __builtin_amdgcn_s_sleep(8);
  }
  __builtin_amdgcn_fence(__ATOMIC_ACQUIRE, "wavefront");
  return v;
}

struct Hint {
  unsigned off; int v;
  __device__ __forceinline__ Hint(unsigned word, int cnt, int lane) : off((word + (unsigned)(lane < cnt ? lane : 0)) * 256u), v(-1) {}
  __device__ __forceinline__ void sample(rsrc_t rw) { asm volatile("" ::: "memory"); v = (int)ld4c(rw, off, WS_CTRL); }
  __device__ __forceinline__ bool has(int s) const { return __all(v >= s); }
  __device__ __forceinline__ void need(rsrc_t rw, int s, bool& dead) {
    for (unsigned sp = 0; !has(s) && !dead; ++sp) {
      if (sp) __builtin_amdgcn_s_sleep(2);
      sample(rw);
      if (sp > SPINCAP) { dead = true; st4c(rw, 0, WS_CTRL + C_TMO * 256, 7000 + s); }
    }
    __builtin_amdgcn_fence(__ATOMIC_ACQUIRE, "wavefront");
  }
};

constexpr int INIT_CAL = 16, INIT_CODE = 16, INIT_FILL = 5;
__global__ void __launch_bounds__(256) k_init(Params p) {
  unsigned char* ws = ws_base(p.ws);
  const int tid = threadIdx.x, lane = tid & 63, w = tid >> 6;
  int b = blockIdx.x;
  if (b < INIT_CAL) {
    if (w) return;
    const unsigned char* a = ws + (5 + 2 * (b & 1)) * MB + 512 * 1024 + b * 16384 + (lane & 31) * 4;
    unsigned best[2] = {~0u, ~0u};
    for (int k = 0; k < 2; ++k)
#pragma unroll
      for (int st = 0; st < 2; ++st) {
        const unsigned char* q = a + st * 8192;
        unsigned long long t0, t1; unsigned val = 0;
        asm volatile("s_memtime %0\n\ts_waitcnt lgkmcnt(0)\n\tglobal_atomic_add %2, %3, %2, off sc0\n\ts_waitcnt vmcnt(0)\n\ts_memtime %1\n\ts_waitcnt lgkmcnt(0)"
                     : "=&s"(t0), "=&s"(t1), "+v"(val) : "v"(q) : "memory");
        if (k) best[st] = min(best[st], (unsigned)(t1 - t0));
      }
    const unsigned flip = (b ^ ((unsigned)__builtin_amdgcn_s_getreg(20 | (3 << 11)) >> 2)) & 1u;
    const int dlt = (int)best[0] - (int)best[1];
    if (lane == 0) *(int*)(ws + WS_CAL + b * 4) = flip ? -dlt : dlt;
    return;
  }
  b -= INIT_CAL;
  if (b < INIT_CODE) {
    unsigned long long a0, a1;
    asm volatile("s_getpc_b64 vcc\n\ts_add_u32 vcc_lo, vcc_lo, _Z6k_main6Params@rel32@lo+4\n\ts_addc_u32 vcc_hi, vcc_hi, _Z6k_main6Params@rel32@hi+12\n\ts_mov_b64 %0, vcc"
                 : "=s"(a0) :: "vcc", "scc");
    asm volatile("s_getpc_b64 vcc\n\ts_add_u32 vcc_lo, vcc_lo, _Z5k_endv@rel32@lo+4\n\ts_addc_u32 vcc_hi, vcc_hi, _Z5k_endv@rel32@hi+12\n\ts_mov_b64 %0, vcc"
                 : "=s"(a1) :: "vcc", "scc");
    const unsigned long long lo = a0 < a1 ? a0 : a1;
    rsrc_t rc = mk_rsrc((const void*)lo, (unsigned)((a0 < a1 ? a1 : a0) - lo));
    const unsigned v = ld4p(rc, ((unsigned)b * 256u + tid) * 128u, 0);
    asm volatile("" :: "v"(v));
    return;
  }
  b -= INIT_CODE;
#pragma unroll
  for (int k = 0; k < 4; ++k) *(v4u*)(ws + WS_CTRL + b * 16384 + k * 4096 + tid * 16) = (v4u){SENT, SENT, SENT, SENT};
}

__device__ __forceinline__ void setup_job(const Params& p, unsigned char* ws, rsrc_t rw, int k0, int lane, int w) {
  const int g = lane >> 4, n = lane & 15;
  const unsigned p0 = stripe_word(rw, lane) >> 3, l16 = lane * 16u;
  const v4u sent = {SENT, SENT, SENT, SENT};
  if (k0 < NSEG) {
    v4u v[2][5]; float mu[2], t[2];
#pragma unroll
    for (int i = 0; i < 10; ++i) v[i & 1][i >> 1] = *(const v4u*)(p.x + (size_t)(k0 * 32 + 16 * (n >> 3) + 8 * (i & 1) + (n & 7)) * D_ + (w * 5 + (i >> 1)) * 32 + g * 8);
    auto total = [&](int slot) {
      if (g == 0) *(LAS f2*)(slot * 512 + w * 128 + n * 8) = (f2){t[0], t[1]};
      bar();
      const unsigned a = slot * 512 + n * 8;
      const f2 r = (*(LAS f2*)a + *(LAS f2*)(a + 128)) + (*(LAS f2*)(a + 256) + *(LAS f2*)(a + 384));
      t[0] = r[0] * (1.f / D_); t[1] = r[1] * (1.f / D_);
    };
#pragma unroll
    for (int rt = 0; rt < 2; ++rt) {
      float sum = 0.f;
#pragma unroll
      for (int e = 0; e < 20; ++e) sum += bflo(v[rt][e >> 2][e & 3]) + bfhi(v[rt][e >> 2][e & 3]);
      t[rt] = xor32_add(xor16_add(sum));
    }
    total(0);
#pragma unroll
    for (int rt = 0; rt < 2; ++rt) {
      mu[rt] = t[rt];
      float sq = 0.f;
#pragma unroll
      for (int e = 0; e < 20; ++e) { const float a = bflo(v[rt][e >> 2][e & 3]) - mu[rt], c = bfhi(v[rt][e >> 2][e & 3]) - mu[rt]; sq += a * a + c * c; }
      t[rt] = xor32_add(xor16_add(sq));
    }
    total(1);
#pragma unroll
    for (int rt = 0; rt < 2; ++rt) {
      const float mub = rbf(mu[rt]), sd = rbf(sqrtf(rbf(rbf(t[rt]) + 1e-5f)));
#pragma unroll
      for (int k = 0; k < 5; ++k) {
        v4u o;
#pragma unroll
        for (int j = 0; j < 4; ++j) o[j] = pk2(rbf(bflo(v[rt][k][j]) - mub) / sd, rbf(bfhi(v[rt][k][j]) - mub) / sd);
        st16c(rw, l16, WS_XF + (unsigned)((k0 * 20 + w * 5 + k) * 2 + rt) * 1024u, o);
      }
    }
  } else {
    for (unsigned i = (k0 - NSEG) * 4 + w; i < 336u * 8u + 80u; i += (NSETUP - NSEG) * 4) {
      v4u v = sent;
      if (i >= HO_MEM / 1024 && i < HO_MEM / 1024 + 10) {
        const unsigned* m32 = (const unsigned*)p.mem0 + (2 * (n >> 2) + (n & 1)) * 320 + 8 * ((i - HO_MEM / 1024) * 4 + g) + ((n >> 1) & 1);
#pragma unroll
        for (int q = 0; q < 4; ++q) v[q] = nosent(m32[2 * q]);
      }
      st16c(rw, l16, i < 336u * 8u ? stripe(WS_HO, i * 1024u, p0) : stripe(WS_PX, (i - 336u * 8u) * 1024u, p0), v);
    }
  }
  VMWAIT(0);
  bar();
  if (w == 0 && lane == 0) __hip_atomic_fetch_add((unsigned*)(ws + WS_CTRL + C_SET1 * 256), 1u, __ATOMIC_RELAXED, __HIP_MEMORY_SCOPE_AGENT);
  if (w == 1 && k0 < 60) STAMP(64 + k0, 2);
}

__device__ __forceinline__ void role_wqk(const Params& p, unsigned char* ws, rsrc_t rw, int j, int lane, int w) {
  const int wu = 4 * j + w;
  const int h = wu < 192 ? wu / 6 : (wu - 192) / 3, qk = wu < 192 ? (wu % 6) / 3 : 2, e3 = wu % 3;
  const int gg = lane >> 4, hi = lane >> 5, r = (lane >> 2) & 3, cc = lane & 3;
  const int dr = lane >> 3, c16 = lane & 7, fs = ((dr >> 1) & 1);
  const unsigned short* W = (qk == 0 ? p.wq : qk == 1 ? p.wk : p.wv) + h * E_ + e3 * 64;
  const unsigned lb = w * 40960;
  auto issue = [&](int blk) {
#pragma unroll
    for (int i = 0; i < 8; ++i) {
      const int f = fs | ((i & 1) << 1);
      glds16(W + (size_t)(blk * 64 + i * 8 + dr) * HE_ + (((c16 >> 1) ^ f) * 16) + (c16 & 1) * 8, lb + (blk % 5) * 8192 + i * 1024);
    }
  };
#pragma unroll
  for (int b = 0; b < 5; ++b) issue(b);
  v4u wt[2][40];
  const int f = (r >> 1) | (hi << 1);
  const unsigned tb0 = opaque(lb + (8 * hi + r) * 128 + (((gg & 1) ^ f) * 32) + cc * 8), tb1 = opaque(lb + (8 * hi + r) * 128 + (((2 + (gg & 1)) ^ f) * 32) + cc * 8);
#pragma unroll
  for (int b = 0; b < 10; ++b) {
    if (b <= 5) VMWAIT(32); else if (b == 6) VMWAIT(24); else if (b == 7) VMWAIT(16); else if (b == 8) VMWAIT(8); else VMWAIT(0);
#pragma unroll
    for (int t = 0; t < 2; ++t)
#pragma unroll
      for (int k = 0; k < 4; ++k) { const unsigned a = (t ? tb1 : tb0) + (b % 5) * 8192 + k * 2048; v2u lo = trread(a), hi2 = trread(a + 512); wt[t][4 * b + k] = (v4u){lo.x, lo.y, hi2.x, hi2.y}; }
    asm volatile("s_waitcnt lgkmcnt(0)" ::: "memory");
    if (b + 5 < 10) issue(b + 5);
  }
#pragma unroll
  for (int k = 0; k < 32; ++k) { PIN_A(wt[0][k]); PIN_A(wt[1][k]); }
  bool dead = false;
  if (w == 1) STAMP(128 + j, 2);
  if (w == 0) { wait_ge(rw, C_SET1, NSETUP - 1, dead); __builtin_amdgcn_fence(__ATOMIC_ACQUIRE, "agent"); VMWAIT(0); }
  bar();
  const unsigned p0 = (stripe_word(rw, lane) >> 3) ^ (qk == 2 ? 1u : 0u);
  const int ofr = (qk == 1 ? 12 : 0) + e3 * 4;
  Hint done(C_TFLAG, 20, lane);
  const int flagword = C_WFLAG + j;
  const unsigned char* xsrc = ws + WS_XF + w * 10240 + lane * 16;
#pragma unroll
  for (int i = 0; i < 10; ++i) glds16(xsrc + i * 1024, w * 10240 + i * 1024);
#pragma unroll
  for (int i = 0; i < 10; ++i) glds16(xsrc + 40960 + i * 1024, 40960 + w * 10240 + i * 1024);
  VMWAIT(0);
  bar();
  const unsigned xo = (gg & 1) * 1024 + hi * 256 + (lane & 15) * 16;
  auto xoff = [](int k) { return (k >> 1) * 2048 + (k & 1) * 512; };
  v4u xr[4];
  xr[0] = lds16(xo); xr[1] = lds16(xo + xoff(1)); xr[2] = lds16(xo + xoff(2));
  f16v accA[2], accB[2];
#pragma unroll
  for (int i = 0; i < 16; ++i) { accB[0][i] = 0.f; accB[1][i] = 0.f; }
  auto put = [&](const f16v (&acc)[2], int q, int s) {
    const f16v a = acc[q >> 1]; const int o = 4 * (q & 1);
    st16c(rw, xo, qk_at(s, h, ofr, p0) + (q >> 1) * 2048 + (q & 1) * 512,
          (v4u){pk2(a[o], a[o + 1]), pk2(a[o + 2], a[o + 3]), pk2(a[o + 8], a[o + 9]), pk2(a[o + 10], a[o + 11])});
  };
  int b0 = 0;
  auto seg = [&](f16v (&acc)[2], const f16v (&prev)[2], int s) {
    const int b1 = b0 == 2 ? 0 : b0 + 1, b2 = b1 == 2 ? 0 : b1 + 1;
    const unsigned cur = opaque(xo + b0 * 40960), nxt = opaque(xo + b1 * 40960), dst2 = b2 * 40960 + w * 10240;
    b0 = b1;
    static_for<40>([&](auto KC) {
      constexpr int k = decltype(KC)::value;
      if constexpr (k == 1) {
        if (w == 0) done.need(rw, s - 1 - min(RING, LEAD0 + max(0, s - LEADS) / 2), dead);
        VMWAIT(0);
        bar();
        if (w == 0 && lane == 0 && s >= 2 + W_EAGER) st4c(rw, 0, WS_CTRL + flagword * 256, (unsigned)(s - 2));
        if (w == 0) { STAMP(flagword - C_WFLAG + 128, 16 + s); STAMPV(flagword - C_WFLAG + 128, 128 + s, (unsigned)__builtin_amdgcn_s_memtime()); }
      }
      mfma_big<k == 0, (k < 32), false>(acc[0], wt[0][k], xr[k & 3]);
      xr[(k + 3) & 3] = lds16(k < 37 ? cur + xoff(k + 3) : nxt + xoff(k - 37));
      SB();
      mfma_big<k == 0, (k < 32), false>(acc[1], wt[1][k], xr[k & 3]);
      SB();
      if constexpr (k >= 1 && k < 9 && (k & 1)) { if (s >= 1 + W_EAGER) put(prev, k >> 1, s - 1); }
      if constexpr (k >= 8 && k < 28 && !(k & 1)) glds16(xsrc + (size_t)(s + 2) * 40960 + ((k - 8) >> 1) * 1024, dst2 + ((k - 8) >> 1) * 1024);
      SB();
    });
    if (s < W_EAGER || s == NSEG - 1) {
      if (s >= RING) done.need(rw, s - RING, dead);
      asm volatile("s_nop 7\n\ts_nop 3" : "+v"(acc[0]), "+v"(acc[1]));
#pragma unroll
      for (int q = 0; q < 4; ++q) put(acc, q, s);
      VMWAIT(0);
      bar();
      if (w == 0 && lane == 0) st4c(rw, 0, WS_CTRL + flagword * 256, (unsigned)s);
    }
  };
  for (int s = 0; s < NSEG; s += 2) { seg(accA, accB, s); seg(accB, accA, s + 1); }
}

__device__ __forceinline__ constexpr int slot_grp(int j) { return j == 0 || (j >= 2 && j < 5) ? 0 : 1; }
__device__ __forceinline__ constexpr int slot_rot(int j) { return j < 2 ? 0 : (j < 5 ? j - 1 : j - 4); }
__device__ __forceinline__ int slot_ks(int j, int w) { return j < 8 ? 4 * slot_grp(j) + ((w + slot_rot(j)) & 3) : j; }
struct MemGather {
  unsigned o0, o1, o2;
  __device__ __forceinline__ MemGather(unsigned hb, int w) : o0(sopaque(hb + ho_phys(HO_MEM + w * 1024))), o1(sopaque(hb + ho_phys(HO_MEM + 4096 + w * 1024))), o2(sopaque(hb + ho_phys(HO_MEM + 8192 + w * 512))) {}
  __device__ __forceinline__ void load(rsrc_t rw, int lane, v4u& a0, v4u& a1, v2u& a2) const {
    a0 = ld16c(rw, lane * 16, o0); SB(); a1 = ld16c(rw, lane * 16, o1); SB(); a2 = __builtin_amdgcn_raw_buffer_load_b64(rw, lane * 8, o2, 16); SB();
  }
  static __device__ __forceinline__ bool bad(v4u a0, v4u a1, v2u a2) { return __any(max(max(mx4(a0), mx4(a1)), max(a2.x, a2.y)) == SENT); }
  __device__ __forceinline__ void late(rsrc_t rw, int lane, v4u& a0, v4u& a1, v2u& a2, bool& dead) const {
    for (unsigned sp = 0; !dead; ++sp) {
      asm volatile("" ::: "memory");
      load(rw, lane, a0, a1, a2);
      if (!bad(a0, a1, a2)) break;
      if (sp > SPINCAP) { dead = true; st4c(rw, 0, WS_CTRL + C_TMO * 256, 1000); }
    }
  }
};
__device__ __forceinline__ void blk_src(int h, int lane, int w, unsigned (&soff)[6], unsigned (&own)[6]) {
#pragma unroll
  for (int i = 0; i < 6; ++i) {
    const int pos = (w * 6 + i) * 64 + lane, d = pos / 24, c16 = pos % 24;
    const int et = (c16 >> 1) ^ (((d >> 1) & 1) | (((d >> 3) & 1) << 1));
    soff[i] = (unsigned)(d * HE_ + h * E_ + et * 16 + (c16 & 1) * 8); own[i] = (unsigned)(et / 3);
  }
}

constexpr unsigned Y_IMG = 122880, Y_KX = Y_IMG + 6144, Y_M = Y_KX + 12288, Y_QX = Y_M + 10240;
__device__ __forceinline__ constexpr int y_blk_qk(int i) { return i < 15 ? (i < 8 ? 0 : 1) : ((i - 15) & 1 ? 0 : 1); }
__device__ __forceinline__ constexpr int y_blk_ks(int i) {
  constexpr int qs[8] = {0, 1, 3, 4, 5, 7, 8, 9};
  constexpr int kk[7] = {1, 2, 3, 5, 6, 7, 9};
  return i < 8 ? qs[i] : (i < 15 ? kk[i - 8] : 2 * (i - 15));
}
__device__ __forceinline__ constexpr int y_tail_t(int j) { constexpr int o[24] = {0, 1, 0, 1, 0, 2, 0, 2, 1, 3, 1, 3, 2, 4, 2, 4, 3, 5, 3, 5, 4, 5, 4, 5}; return o[j]; }
__device__ __forceinline__ constexpr int y_tail_k(int j) { int c = 0; for (int i = 0; i < j; ++i) if (y_tail_t(i) == y_tail_t(j)) ++c; return 6 + c; }
__device__ __forceinline__ constexpr int y_find(int qk, int ks) { for (int i = 0; i < 20; ++i) if (y_blk_qk(i) == qk && y_blk_ks(i) == ks) return i; return -1; }

__device__ __forceinline__ void role_y(const Params& p, unsigned char* ws, rsrc_t rw, int h, int lane, int w) {
  const int g = lane >> 4, n = lane & 15, r = (lane >> 2) & 3, cc = lane & 3;
  bool dead = false;
  unsigned soff[6], own[6];
  blk_src(h, lane, w, soff, own);
  auto issue = [&](int i) {
    const unsigned short* W = y_blk_qk(i) ? p.wk : p.wq;
#pragma unroll
    for (int q = 0; q < 6; ++q) glds16(W + soff[q] + (unsigned)slot_ks(y_blk_ks(i), own[q]) * (64u * HE_), (i % 5) * 24576 + (w * 6 + q) * 1024);
  };
  unsigned tb[3];
  { const int f = (r >> 1) | ((g & 1) << 1);
#pragma unroll
    for (int t = 0; t < 3; ++t) tb[t] = (8 * g + r) * 384 + (((3 * w + t) ^ f) * 32) + cc * 8; }
  auto rdfrag = [&](int slot, int t) -> v8u {
    const unsigned a = tb[t] + slot * 24576;
    v2u q0 = trread(a), q1 = trread(a + 4 * 384), q2 = trread(a + 32 * 384), q3 = trread(a + 36 * 384);
    return (v8u){q0.x, q0.y, q1.x, q1.y, q2.x, q2.y, q3.x, q3.y};
  };
#pragma unroll
  for (int i = 0; i < 5; ++i) issue(i);
  v8u wt[15][3];
#pragma unroll
  for (int i = 0; i < 15; ++i) {
    VMWAIT(24);
    bar();
#pragma unroll
    for (int t = 0; t < 3; ++t) wt[i][t] = rdfrag(i % 5, t);
    bar();
#pragma unroll
    for (int t = 0; t < 3; ++t) if (i * 3 + t < 32) PIN_A(wt[i][t]);
    issue(i + 5);
  }
  VMWAIT(0);
  const unsigned pol = stripe_word(rw, lane);
  const unsigned p0 = pol >> 3;
  if (w == 1) STAMP(h, 5);
  wait_ge(rw, C_SET1, NSETUP - 1, dead);
  const int jf0 = C_WFLAG + (6 * h) / 4;
  int seen = -1;
  auto need_qk = [&](int s) {
    for (unsigned sp = 0; seen < s && !dead; ++sp) {
      asm volatile("" ::: "memory");
      const int v = (int)ld4c(rw, (lane & 1) * 256, WS_CTRL + jf0 * 256);
      seen = min(__builtin_amdgcn_readlane(v, 0), __builtin_amdgcn_readlane(v, 1));
      if (sp > SPINCAP) { dead = true; st4c(rw, 0, WS_CTRL + C_TMO * 256, 1500 + s); }
      if (seen < s) __builtin_amdgcn_s_sleep(2);
    }
    __builtin_amdgcn_fence(__ATOMIC_ACQUIRE, "wavefront");
  };
  const unsigned kxb = opaque(Y_KX + lane * 16);
  const unsigned kxp = opaque(Y_KX + (g * 16 + (n & 8) + 4 * (n & 1) + 2 * ((n >> 2) & 1) + ((n >> 1) & 1)) * 16);
  auto load_kx = [&](int s, v4u (&t)[12]) {
#pragma unroll
    for (int i = 0; i < 12; ++i) t[i] = ld16c(rw, lane * 16, qk_at(s, h, 12 + (i & ~3), p0) + (i & 3) * 1024);
  };
  const unsigned qxb = opaque(Y_QX + ((w - 1) & 1) * 6144 + lane * 16);
  auto load_qx = [&](int s, v4u (&q)[6]) {
#pragma unroll
    for (int u = 0; u < 6; ++u) q[u] = ld16c(rw, (n >> 3) * 1024 + (g * 16 + 8 * (w - 1) + (n & 7)) * 16, qk_at(s, h, 4 * (u >> 1), p0) + (u & 1) * 2048);
  };
  if (w == 3) need_qk(1);
  bar();
  const int sidx = sparse_idx(lane);
  const float cexp = 0.07216878364870322f * 1.4426950408889634f;
  const unsigned imwk = opaque(Y_IMG + 3072 + 3 * w * 256 + n * 16 + g * 4);
  unsigned imwq[3];
#pragma unroll
  for (int t = 0; t < 3; ++t) { const int et = 3 * w + t;
    imwq[t] = opaque(Y_IMG + (et >> 2) * 1024 + ((2 * ((et >> 1) & 1) + (n >> 3)) * 16 + 4 * g + 2 * ((n >> 1) & 1)) * 16 + (2 * ((n >> 2) & 1) + (et & 1)) * 4 + (n & 1) * 2); }
  const unsigned imr = opaque(Y_IMG + 3072 + (4 * g + r) * 16 + (cc & 1) * 8), imq = opaque(Y_IMG + lane * 16);
  const unsigned mw = opaque(Y_M + w * 1024 + lane * 16), mw2 = opaque(Y_M + 8192 + w * 512 + lane * 8);
  unsigned mr[4];
#pragma unroll
  for (int i = 1; i < 4; ++i) mr[i] = opaque(Y_M + ((w + i) & 3) * 1024 + lane * 16);
  mr[0] = opaque(Y_M + 8192 + lane * 16);
  unsigned tb2[3];
#pragma unroll
  for (int t = 0; t < 3; ++t) tb2[t] = opaque(tb[t] + 3 * 24576);
  STAMP(h, 1);
  STAMPV(h, 2, (unsigned)__builtin_amdgcn_s_memtime());
  unsigned tpass = rclk();
  bool first = true; unsigned c0 = 0, tb0 = 0, cy[8] = {0, 0, 0, 0, 0, 0, 0, 0}; (void)tb0; (void)first;
  auto next_pace = [&]() {
    const unsigned e = __builtin_amdgcn_readfirstlane(lds4(Y_M + w * 1024)); tpass = (e >> 8) + (e & 255u); };
  auto proj = [&](auto FIN, unsigned hb, v4u (&kx)[12], auto&& before, auto&& shadow) {
    constexpr bool fin = decltype(FIN)::value;
    v4u a[10]; v2u a2; f4 acc[6]; v2u sa[3][4], sb[3][4];
    auto cat = [](const v2u (&q)[4]) -> v8u { return (v8u){q[0].x, q[0].y, q[1].x, q[1].y, q[2].x, q[2].y, q[3].x, q[3].y}; };
    auto rdq = [&](int slot, int t, int q) -> v2u {
      return trread((slot < 3 ? tb[t] + slot * 24576 : tb2[t] + (slot - 3) * 24576) + (32 * (q >> 1) + 4 * (q & 1)) * 384); };
    auto rda = [&](int j) -> v4u { return j < 8 ? lds16(mr[slot_rot(j)] + slot_grp(j) * 4096) : lds16(mr[0] + (j - 8) * 1024); };
    auto zero = [&]() {
#pragma unroll
      for (int t = 0; t < 6; ++t) { acc[t] = (f4){0, 0, 0, 0}; asm volatile("" : "+v"(acc[t])); } };
    auto mm = [&](auto GC) {
      constexpr int G = decltype(GC)::value;
      constexpr int ks = G < 36 ? G / 6 : y_tail_k(G < 36 ? 0 : G - 36), t = G < 36 ? G % 6 : y_tail_t(G < 36 ? 0 : G - 36);
      constexpr int bi = y_find(t / 3, ks);
      if constexpr (bi < 15) acc[t] = smfmac64(a[ks], wt[bi][t % 3], acc[t], sidx);
      else acc[t] = smfmac64(a[ks], t < 3 ? cat(sb[t % 3]) : cat(sa[t % 3]), acc[t], sidx);
      SB();
    };
    before();
    SB();
#pragma unroll
    for (int i = 0; i < 12; ++i) sa[i >> 2][i & 3] = rdq(0, i >> 2, i & 3);
    zero();
    const MemGather mg(hb, w);
    SB();
    wait_until(tpass);
    CYC(cy[0]);
    SB();
    mg.load(rw, lane, a[0], a[1], a2);
    c0 = cclk();
    shadow();
    SB();
#pragma unroll
    for (int i = 0; i < 12; ++i) sb[i >> 2][i & 3] = rdq(1, i >> 2, i & 3);
    SB();
    lds16w(mw, a[0]);
    SB();
    mm(std::integral_constant<int, 0>{});
    lds16w(mw + 4096, a[1]);
    SB();
    mm(std::integral_constant<int, 1>{});
    *(LAS v2u*)mw2 = a2;
    SB();
    mm(std::integral_constant<int, 2>{});
    unsigned mx = max(mx4(a[0]), a2.x); asm volatile("" : "+v"(mx)); SB();
    mm(std::integral_constant<int, 3>{});
    mx = max(max(mx, a2.y), mx4(a[1])); asm volatile("" : "+v"(mx)); SB();
    mm(std::integral_constant<int, 4>{});
    first = !__any(mx == SENT);
    mm(std::integral_constant<int, 5>{});
    if (__builtin_expect(!first, 0)) {
      mg.late(rw, lane, a[0], a[1], a2, dead);
      lds16w(mw, a[0]); lds16w(mw + 4096, a[1]); *(LAS v2u*)mw2 = a2;
      zero();
      static_for<6>(mm);
    }
    CYC(cy[1]);
    VMWAIT(0);
    bar();
    CYC(cy[2]);
    SB();
    f2 fs = {0.f, 0.f}; unsigned f0 = 0;
    static_for<54>([&](auto GC0) {
      constexpr int G = decltype(GC0)::value + 6;
      if constexpr (G < 14) a[G - 4] = rda(G - 4);
      else if constexpr (G < 24) { sa[(G - 14) >> 2][(G - 14) & 3] = rdq(2, (G - 14) >> 2, (G - 14) & 3); if constexpr (G >= 22) sa[2][G - 20] = rdq(2, 2, G - 20); }
      else if constexpr (G < 36) sb[(G - 24) >> 2][(G - 24) & 3] = rdq(3, (G - 24) >> 2, (G - 24) & 3);
      else if constexpr (G < 48) sa[(G - 36) >> 2][(G - 36) & 3] = rdq(4, (G - 36) >> 2, (G - 36) & 3);
      else if constexpr (fin) kx[G - 48] = lds16(kxp + (G - 48) * 1024);
      if constexpr (G >= 45) {
        constexpr int tp = (G - 45) / 4, ph = (G - 45) % 4;
        if constexpr (ph == 0) { fs = (f2){acc[tp][0], acc[tp][1]} + (f2){acc[tp][2], acc[tp][3]}; asm volatile("" : "+v"(fs)); }
        if constexpr (ph == 1) { f0 = pk2(fs[0], fs[1]); asm volatile("" : "+v"(f0)); }
        if constexpr (ph == 2) { if constexpr (tp < 3) *(LAS unsigned short*)imwq[tp < 3 ? tp : 0] = (unsigned short)f0; else lds4w(imwk, f0); }
        if constexpr (ph == 3 && tp < 3) *(LAS unsigned short*)(imwq[tp < 3 ? tp : 0] + 16) = (unsigned short)(f0 >> 16);
      }
      SB();
      mm(std::integral_constant<int, G>{});
    });
    CYC(cy[3]);
    lds4w(imwk + 256, fold_pack(acc[4]));
    lds4w(imwk + 512, fold_pack(acc[5]));
    bar();
  };
  if (w == 0) {
    const unsigned pso = (n >> 3) * 256 + (4 * g + 2 * ((n >> 1) & 1) + ((n >> 2) & 1)) * 16 + (n & 1) * 8, pmo = (4 * g + 2 * ((n >> 1) & 1) + (n >> 3)) * 8 + ((n >> 2) & 1) * 4 + (n & 1) * 2;
    for (int s = 0; s < NSEG; ++s) {
      const unsigned hb = ho_base(s, pol);
      const unsigned ox = sopaque(hb + ho_phys(HO_P + (32 + 4 * h) * 128)), om = sopaque(hb + ho_phys(HO_P + h * 128));
      v4u kx[12];
      f4 d0 = {0, 0, 0, 0}, d1 = d0, d2 = d0;
      proj(std::true_type{}, hb, kx, []() {}, []() {});
      CYC(cy[4]);
      v4u qa[3]; v2u kr[12];
#pragma unroll
      for (int v = 0; v < 3; ++v) qa[v] = lds16(imq + v * 1024);
#pragma unroll
      for (int i = 0; i < 12; ++i) kr[i] = trread(imr + i * 256);
      asm volatile("" : "+v"(d0), "+v"(d1), "+v"(d2));
      SB();
#pragma unroll
      for (int v = 0; v < 3; ++v) {
        const v4u l0 = kx[4 * v], h0 = kx[4 * v + 2], l1 = kx[4 * v + 1], h1 = kx[4 * v + 3];
        d1 = smfmac64(qa[v], (v8u){l0.x, l0.y, l0.z, l0.w, h0.x, h0.y, h0.z, h0.w}, d1, sidx);
        d2 = smfmac64(qa[v], (v8u){l1.x, l1.y, l1.z, l1.w, h1.x, h1.y, h1.z, h1.w}, d2, sidx);
        SB();
      }
#pragma unroll
      for (int v = 0; v < 3; ++v)
        d0 = smfmac64(qa[v], (v8u){kr[4 * v].x, kr[4 * v].y, kr[4 * v + 1].x, kr[4 * v + 1].y, kr[4 * v + 2].x, kr[4 * v + 2].y, kr[4 * v + 3].x, kr[4 * v + 3].y}, d0, sidx);
      SB();
      CYC(cy[5]);
      const f2 x0 = (f2){d1[0], d1[1]} + (f2){d1[2], d1[3]}, x1 = (f2){d2[0], d2[1]} + (f2){d2[2], d2[3]}, xm = (f2){d0[0], d0[1]} + (f2){d0[2], d0[3]};
      float ma = vmax3(x0[0], x1[0], xm[0]), mb = vmax3(x0[1], x1[1], xm[1]);
      row16_max(ma, mb);
      const float na = -ma * cexp, nb = -mb * cexp;
      const float e0a = __builtin_amdgcn_exp2f(__builtin_fmaf(x0[0], cexp, na)), e0b = __builtin_amdgcn_exp2f(__builtin_fmaf(x0[1], cexp, nb));
      const float e1a = __builtin_amdgcn_exp2f(__builtin_fmaf(x1[0], cexp, na)), e1b = __builtin_amdgcn_exp2f(__builtin_fmaf(x1[1], cexp, nb));
      const float ema = __builtin_amdgcn_exp2f(__builtin_fmaf(xm[0], cexp, na)), emb = __builtin_amdgcn_exp2f(__builtin_fmaf(xm[1], cexp, nb));
      float sa_ = __builtin_fmaf(ema, 0.5f, e0a + e1a), sb_ = __builtin_fmaf(emb, 0.5f, e0b + e1b);
      const float ems = (n & 8) ? emb : ema;
      row16_add(sa_, sb_);
      const float ra = __builtin_amdgcn_rcpf(sa_), rb = __builtin_amdgcn_rcpf(sb_);
      {
        const unsigned pa = pk2a(e0a * ra, e1a * ra), pb2 = pk2a(e0b * rb, e1b * rb);
        const unsigned lo = (unsigned)__builtin_amdgcn_update_dpp((int)pa, (int)pb2, 0x114, 0xf, 0xa, false);
        const unsigned hi = (unsigned)__builtin_amdgcn_update_dpp((int)pb2, (int)pa, 0x104, 0xf, 0x5, false);
        st8c(rw, pso, ox, (v2u){lo, hi});
      }
      st2c(rw, pmo, om, (unsigned short)pk2a(ems * ((n & 8) ? rb : ra), 0.f));
      SB();
      if (lane == 0) report(rw, h, s, cclk() - c0, p0);
      CYC(cy[6]);
      STAMP(h, 16 + s * 8 + 4);
      bar();
      next_pace();
    }
  } else if (w == 3) {
    v4u t[12];
    load_kx(0, t);
    for (int s = 0; s < NSEG; ++s) {
      const unsigned hb = ho_base(s, pol);
      proj(std::false_type{}, hb, t, [&]() {
#pragma unroll
        for (int i = 0; i < 12; ++i) lds16w(kxb + i * 1024, t[i]); }, []() {});
      unsigned e = ld4c(rw, 0, hb + ho_phys(HO_E));
      SB();
      load_kx(s + 1 < NSEG ? s + 1 : s, t);
      SB();
      e = __builtin_amdgcn_readfirstlane(e);
      if (e == SENT && s + 1 < NSEG) e = wait_word(rw, hb + ho_phys(HO_E), dead);
      if (lane < 4) lds4w(Y_M + lane * 1024, e);
      STAMP(h, 16 + s * 8 + 3); STAMPV(h, 16 + s * 8 + 7, first ? 0 : 1); STAMPV(h, 16 + s * 8, tpass);
      bar();
      next_pace();
      need_qk(s + 2 < NSEG ? s + 2 : NSEG - 1);
    }
  } else {
    const int rq = w - 1, tok = 16 * rq + n;
    v4u qn[6];
    load_qx(0, qn);
    for (int s = 0; s < NSEG; ++s) {
      const unsigned hb = ho_base(s, pol);
      v4u t[12];
      proj(std::false_type{}, hb, t, []() {}, [&]() {
#pragma unroll
        for (int u = 0; u < 6; ++u) lds16w(qxb + u * 1024, qn[u]); });
      __builtin_amdgcn_s_sleep(2);
      f4 d0, d1, d2;
      {
        v4u qf[6]; v2u kr[12];
#pragma unroll
        for (int u = 0; u < 6; ++u) { qf[u] = lds16(qxb + u * 1024); t[2 * u] = lds16(kxb + 2 * u * 1024); t[2 * u + 1] = lds16(kxb + (2 * u + 1) * 1024); }
#pragma unroll
        for (int u = 0; u < 6; ++u) {
          if (u == 0) { mfma_v0(d1, t[0], qf[0]); mfma_v0(d2, t[1], qf[0]); } else { mfma_v(d1, t[2 * u], qf[u]); mfma_v(d2, t[2 * u + 1], qf[u]); }
          kr[2 * u] = trread(imr + 2 * u * 256); kr[2 * u + 1] = trread(imr + (2 * u + 1) * 256);
          SB();
        }
#pragma unroll
        for (int u = 0; u < 6; ++u) {
          const v4u kf = {kr[2 * u].x, kr[2 * u].y, kr[2 * u + 1].x, kr[2 * u + 1].y};
          if (u == 0) mfma_v0(d0, kf, qf[0]); else mfma_v(d0, kf, qf[u]);
        }
        asm volatile("s_nop 7" : "+v"(d0), "+v"(d1), "+v"(d2));
      }
      bar();
      load_qx(s + 1 < NSEG ? s + 1 : s, qn);
      if (s + 1 < NSEG) {
        if (rq || g < 2) st16c(rw, lane * 16, px_at(s + 1, h * 2 + rq, p0), (v4u){SENT, SENT, SENT, SENT});
        st4c(rw, (h & 3) * 256 + lane * 4, px_at(s + 1, (32 + (h >> 2)) * 2 + rq, p0), SENT);
      }
      SB();
      next_pace();
      float v[10];
      const float NEG = -1.0e9f * 1.4426950408889634f;
#pragma unroll
      for (int i = 0; i < 4; ++i) { const int key = 16 * (g >> 1) + 4 * (g & 1) + i; v[i] = (key <= tok) ? d1[i] * cexp : NEG; v[4 + i] = (key + 8 <= tok) ? d2[i] * cexp : NEG; }
      v[8] = ((g & 2) ? d0[2] : d0[0]) * cexp; v[9] = ((g & 2) ? d0[3] : d0[1]) * cexp;
      float m = vmax3(vmax3(v[0], v[1], v[2]), vmax3(v[3], v[4], v[5]), vmax3(vmax3(v[6], v[7], v[8]), v[9], v[9]));
      m = xor32_max(xor16_max(m));
      float sum = 0.f;
#pragma unroll
      for (int i = 0; i < 10; ++i) { v[i] = __builtin_amdgcn_exp2f(v[i] - m); sum += v[i]; }
      sum = xor32_add(xor16_add(sum));
      const float rs = __builtin_amdgcn_rcpf(sum);
      v4u px;
#pragma unroll
      for (int i = 0; i < 4; ++i) px[i] = pk2a(v[i] * rs, v[4 + i] * rs);
      if (rq || g < 2) st16c(rw, lane * 16, px_at(s, h * 2 + rq, p0), px);
      st4c(rw, ((h & 3) * 16 + n) * 16 + (2 * (g & 1) + (g >> 1)) * 4, px_at(s, (32 + (h >> 2)) * 2 + rq, p0), pk2a(v[8] * rs, v[9] * rs));
      STAMP(h, 16 + s * 8 + 4 + w);
    }
  }
  STAMP(h, 4);
  STAMPV(h, 3, (unsigned)__builtin_amdgcn_s_memtime());
}

template <bool PERM>
__device__ __forceinline__ void load_wo(const Params& p, int h, int lane, int w, v4u (&wo)[10][6]) {
  const int g = lane >> 4, r = (lane >> 2) & 3, cc = lane & 3;
  const unsigned lb = w * 30720;
  auto issue = [&](int u) {
#pragma unroll
    for (int i = 0; i < 10; ++i) {
      const int pos = i * 64 + lane, e = pos / 20, c16 = pos % 20;
      glds16(p.wo + (size_t)(h * E_ + u * 32 + e) * D_ + w * 160 + c16 * 8, lb + (u % 3) * 10240 + i * 1024);
    }
  };
  issue(0); issue(1); issue(2);
  const unsigned base = opaque(lb + (PERM ? 4 * g + r : 8 * g + r) * 320 + cc * 8);
#pragma unroll
  for (int u = 0; u < 6; ++u) {
    if (u <= 3) VMWAIT(20); else if (u == 4) VMWAIT(10); else VMWAIT(0);
#pragma unroll
    for (int t = 0; t < 10; ++t) {
      const unsigned a = base + (u % 3) * 10240 + t * 32;
      v2u lo = trread(a), hi = trread(a + (PERM ? 16 : 4) * 320);
      wo[t][u] = (v4u){lo.x, lo.y, hi.x, hi.y};
    }
    asm volatile("s_waitcnt lgkmcnt(0)" ::: "memory");
    if (u + 3 < 6) issue(u + 3);
  }
}

__device__ __forceinline__ void role_v2(const Params& p, unsigned char* ws, rsrc_t rw, int h, int lane, int w) {
  const int gg = lane >> 4, hi = lane >> 5, r = (lane >> 2) & 3, cc = lane & 3;
  v4u wo[5][12];
  {
    const unsigned lb = w * 30720;
    auto issue = [&](int u) {
#pragma unroll
      for (int i = 0; i < 10; ++i) {
        const int pos = i * 64 + lane, e = pos / 20, c16 = pos % 20;
        glds16(p.wo + (size_t)(h * E_ + u * 32 + e) * D_ + w * 160 + c16 * 8, lb + (u % 3) * 10240 + i * 1024);
      }
    };
    issue(0); issue(1); issue(2);
    const unsigned base = opaque(lb + (4 * hi + r) * 320 + (gg & 1) * 32 + cc * 8);
#pragma unroll
    for (int u = 0; u < 6; ++u) {
      if (u <= 3) VMWAIT(20); else if (u == 4) VMWAIT(10); else VMWAIT(0);
#pragma unroll
      for (int t = 0; t < 5; ++t)
#pragma unroll
        for (int k = 0; k < 2; ++k) { const unsigned a = base + (u % 3) * 10240 + t * 64 + k * 8 * 320; v2u lo = trread(a), hi2 = trread(a + 16 * 320); wo[t][2 * u + k] = (v4u){lo.x, lo.y, hi2.x, hi2.y}; }
      asm volatile("s_waitcnt lgkmcnt(0)" ::: "memory");
      if (u + 3 < 6) issue(u + 3);
    }
  }
#pragma unroll
  for (int t = 0; t < 5; ++t)
#pragma unroll
    for (int k = 0; k < 12; ++k) PIN_A(wo[t][k]);
  bar();
  bool dead = false;
  const int jf0 = C_WFLAG + 48 + (3 * h) / 4, jf1 = C_WFLAG + 48 + (3 * h + 2) / 4;
  const unsigned fo = ((lane & 1) ? jf1 : jf0) * 256u;
  int fl = -1;
  auto need = [&](int s) {
    for (unsigned sp = 0; !__all(fl >= s) && !dead; ++sp) {
      asm volatile("" ::: "memory");
      if (sp) __builtin_amdgcn_s_sleep(2);
      fl = (int)ld4c(rw, fo, WS_CTRL);
      if (sp > SPINCAP) { dead = true; st4c(rw, 0, WS_CTRL + C_TMO * 256, 2500 + s); }
    }
    __builtin_amdgcn_fence(__ATOMIC_ACQUIRE, "wavefront");
  };
  const unsigned l16 = lane * 16u, vw = opaque(w * 3072 + l16), vr = opaque((gg & 1) * 1024 + hi * 256 + (lane & 15) * 16);
  const unsigned uo = (gg & 1) * 65536u + hi * 256 + (lane & 15) * 16;
  v4u vn[3], va[12], oa[10], ob[10];
  const unsigned p0 = stripe_word(rw, lane) >> 3;
  auto ldv = [&](int s, int i) { vn[i] = ld16c(rw, l16, qk_at(s, h, 3 * w + i, p0 ^ 1u)); };
  auto put = [&](const v4u (&o)[10], int q, int s) { st16c(rw, uo, ux_at(s, w * 10 + (q & ~1), h, p0) + (q & 1) * 512, o[q]); };
#pragma unroll
  for (int t = 0; t < 10; ++t) ob[t] = (v4u){0, 0, 0, 0};
  auto fetch = [&](int s) {
    need(s);
#pragma unroll
    for (int i = 0; i < 3; ++i) ldv(s, i);
#pragma unroll
    for (int i = 0; i < 3; ++i) lds16w(vw + i * 1024, vn[i]);
    bar();
#pragma unroll
    for (int k = 0; k < 12; ++k) va[k] = lds16(vr + (k >> 1) * 2048 + (k & 1) * 512);
    bar();
  };
  fetch(0);
  auto seg = [&](v4u (&out)[10], const v4u (&prev)[10], int s) {
    if (w == 0) { STAMP(200 + h, 16 + s); STAMPV(200 + h, 128 + s, (unsigned)__builtin_amdgcn_s_memtime()); }
    const int sn = s + 1 < NSEG ? s + 1 : s;
    f16v acc[2];
    auto pack = [&](int t, int q) { const f16v a = acc[t & 1]; const int o = 4 * q;
      out[2 * t + q] = (v4u){pk2(a[o], a[o + 8]), pk2(a[o + 1], a[o + 9]), pk2(a[o + 2], a[o + 10]), pk2(a[o + 3], a[o + 11])}; asm volatile("" : "+v"(out[2 * t + q])); };
    static_for<60>([&](auto GC) {
      constexpr int G = decltype(GC)::value, t = G / 12, k = G % 12;
      if constexpr (G == 42) { if (s >= V_EAGER) { need(sn); ldv(sn, 0); ldv(sn, 1); ldv(sn, 2); } }
      mfma_big<k == 0, false, true>(acc[t & 1], va[k], wo[t][k]);
      SB();
      if constexpr (t >= 1 && (k == 3 || k == 5)) pack(t - 1, k == 5);
      if constexpr (G < 30 && G % 3 == 0) { if (s > V_EAGER) put(prev, G / 3, s - 1); }
      if constexpr (G == 2) { if (!__all(fl >= sn)) fl = (int)ld4c(rw, fo, WS_CTRL); }
      SB();
    });
    VMWAIT(0);
#pragma unroll
    for (int i = 0; i < 3; ++i) lds16w(vw + (sn & 1) * 12288 + i * 1024, vn[i]);
    bar();
    if (w == 0 && lane == 0 && s > V_EAGER) st4c(rw, 0, WS_CTRL + (C_UFLAG + h) * 256, (unsigned)(s - 1));
#pragma unroll
    for (int k = 0; k < 12; ++k) va[k] = lds16(vr + (sn & 1) * 12288 + (k >> 1) * 2048 + (k & 1) * 512);
    PAD_BIG();
    pack(4, 0); pack(4, 1);
    if (s < V_EAGER || s == NSEG - 1) {
#pragma unroll
      for (int t = 0; t < 10; ++t) put(out, t, s);
      VMWAIT(0);
      bar();
      if (w == 0 && lane == 0) st4c(rw, 0, WS_CTRL + (C_UFLAG + h) * 256, (unsigned)s);
      if (s < V_EAGER) fetch(s + 1);
    }
  };
  for (int s = 0; s < NSEG; s += 2) { seg(oa, ob, s); seg(ob, oa, s + 1); }
}

constexpr unsigned V_IMG = 122880, V_M = V_IMG + 3072, V_T = V_M + 10240;
__device__ __forceinline__ void role_v(const Params& p, unsigned char* ws, rsrc_t rw, int h, int lane, int w) {
  const int g = lane >> 4, n = lane & 15, r = (lane >> 2) & 3, cc = lane & 3;
  bool dead = false;
  v4u wo[10][6];
  load_wo<false>(p, h, lane, w, wo);
  v8u w2[10][3];
#pragma unroll
  for (int t = 0; t < 10; ++t)
#pragma unroll
    for (int k = 0; k < 3; ++k) { const v4u lo = wo[t][2 * k], hi = wo[t][2 * k + 1]; w2[t][k] = (v8u){lo.x, lo.y, lo.z, lo.w, hi.x, hi.y, hi.z, hi.w}; PIN_A(w2[t][k]); }
  bar();
  unsigned soff[6], own[6];
  blk_src(h, lane, w, soff, own);
  auto issue = [&](int i) {
#pragma unroll
    for (int q = 0; q < 6; ++q) glds16(p.wv + soff[q] + (unsigned)slot_ks(i < 5 ? 2 * i + 1 : 2 * (i - 5), own[q]) * (64u * HE_), (i % 5) * 24576 + (w * 6 + q) * 1024);
  };
  unsigned tb[3], tb2[3];
  { const int f = (r >> 1) | ((g & 1) << 1);
#pragma unroll
    for (int t = 0; t < 3; ++t) { tb[t] = opaque((8 * g + r) * 384 + (((3 * w + t) ^ f) * 32) + cc * 8); tb2[t] = opaque(tb[t] + 3 * 24576); } }
  auto rdq = [&](int slot, int t, int q) -> v2u {
    return trread((slot < 3 ? tb[t] + slot * 24576 : tb2[t] + (slot - 3) * 24576) + (32 * (q >> 1) + 4 * (q & 1)) * 384); };
  auto cat = [](const v2u (&q)[4]) -> v8u { return (v8u){q[0].x, q[0].y, q[1].x, q[1].y, q[2].x, q[2].y, q[3].x, q[3].y}; };
#pragma unroll
  for (int i = 0; i < 5; ++i) issue(i);
  v8u w1[5][3];
#pragma unroll
  for (int i = 0; i < 5; ++i) {
    VMWAIT(24);
    bar();
#pragma unroll
    for (int t = 0; t < 3; ++t) { v2u q[4];
#pragma unroll
      for (int k = 0; k < 4; ++k) q[k] = rdq(i, t, k);
      w1[i][t] = cat(q); }
    bar();
    if (i == 0) { PIN_A(w1[0][0]); PIN_A(w1[0][1]); }
    issue(i + 5);
  }
  VMWAIT(0);
  const unsigned pol = stripe_word(rw, lane);
  wait_ge(rw, C_SET1, NSETUP - 1, dead);
  bar();
  const int sidx = sparse_idx(lane);
  const unsigned imw = opaque(V_IMG + (4 * g + 2 * ((n >> 1) & 1)) * 16 + (n >> 2) * 4 + (n & 1) * 2 + 3 * w * 256), imr = opaque(V_IMG + lane * 16);
  const unsigned mw = opaque(V_M + w * 1024 + lane * 16), mw2 = opaque(V_M + 8192 + w * 512 + lane * 8);
  unsigned mr[4];
#pragma unroll
  for (int i = 1; i < 4; ++i) mr[i] = opaque(V_M + ((w + i) & 3) * 1024 + lane * 16);
  mr[0] = opaque(V_M + 8192 + lane * 16);
  const unsigned umo = h * 256 + n * 16 + g * 16384, umo2 = g >= 2 ? umo : 0xF0000000u;
  STAMP(32 + h, 1);
  unsigned tpass = rclk();
  for (int s = 0; s < NSEG; ++s) {
    const unsigned hb = ho_base(s, pol);
    bool first; unsigned cy[8] = {0, 0, 0, 0, 0, 0, 0, 0}; (void)cy;
    const unsigned ub = sopaque(WS_UM + (unsigned)((s & 1) * 40 + w * 10 - 2) * 16384u + (((pol >> 2) & 1) << 13));
    f4 ac2[10]; unsigned c0;
    {
      v4u a[10]; v2u a2; f4 acc[3]; v2u sa[3][4], sb[3][4], sc[3][4];
      auto zero = [&]() {
#pragma unroll
        for (int t = 0; t < 3; ++t) { acc[t] = (f4){0, 0, 0, 0}; asm volatile("" : "+v"(acc[t])); } };
      auto mm = [&](auto GC) {
        constexpr int G = decltype(GC)::value, i = G / 3, t = G % 3, j = i < 6 ? i : (i == 6 ? 7 : i == 7 ? 9 : i == 8 ? 6 : 8);
        if constexpr (j & 1) acc[t] = smfmac64(a[j], w1[j >> 1][t], acc[t], sidx);
        else acc[t] = smfmac64(a[j], (j == 0 || j == 6) ? cat(sa[t]) : (j == 4 ? cat(sc[t]) : cat(sb[t])), acc[t], sidx);
        SB();
      };
      const MemGather mg(hb, w);
      SB();
      wait_until(tpass);
      CYC(cy[0]);
      SB();
      mg.load(rw, lane, a[0], a[1], a2);
      c0 = cclk();
#pragma unroll
      for (int i = 0; i < 12; ++i) { sa[i >> 2][i & 3] = rdq(0, i >> 2, i & 3); sb[i >> 2][i & 3] = rdq(1, i >> 2, i & 3); sc[i >> 2][i & 3] = rdq(2, i >> 2, i & 3); }
      zero();
      SB();
      lds16w(mw, a[0]); SB();
      lds16w(mw + 4096, a[1]); SB();
      *(LAS v2u*)mw2 = a2; SB();
      mm(std::integral_constant<int, 0>{});
      first = !MemGather::bad(a[0], a[1], a2);
      mm(std::integral_constant<int, 1>{});
      if (__builtin_expect(!first, 0)) {
        mg.late(rw, lane, a[0], a[1], a2, dead);
        lds16w(mw, a[0]); lds16w(mw + 4096, a[1]); *(LAS v2u*)mw2 = a2;
        zero();
        static_for<2>(mm);
      }
      CYC(cy[1]);
      bar();
      CYC(cy[2]);
      SB();
      static_for<28>([&](auto GC0) {
        constexpr int G = decltype(GC0)::value + 2;
        auto rda = [&](int j) -> v4u { return j < 8 ? lds16(mr[slot_rot(j)] + slot_grp(j) * 4096) : lds16(mr[0] + (j - 8) * 1024); };
        if constexpr (G == 2) { a[2] = rda(2); a[3] = rda(3); a[4] = rda(4); }
        else if constexpr (G < 8) a[G == 3 ? 5 : G == 4 ? 7 : G == 5 ? 9 : G == 6 ? 6 : 8] = rda(G == 3 ? 5 : G == 4 ? 7 : G == 5 ? 9 : G == 6 ? 6 : 8);
        else if constexpr (G < 16) { constexpr int i0 = (3 * (G - 8)) / 2, i1 = (3 * (G - 7)) / 2;
          sa[i0 >> 2][i0 & 3] = rdq(3, i0 >> 2, i0 & 3); if constexpr (i1 - i0 == 2) sa[(i0 + 1) >> 2][(i0 + 1) & 3] = rdq(3, (i0 + 1) >> 2, (i0 + 1) & 3); }
        else if constexpr (G < 24) { constexpr int i0 = (3 * (G - 16)) / 2, i1 = (3 * (G - 15)) / 2;
          sb[i0 >> 2][i0 & 3] = rdq(4, i0 >> 2, i0 & 3); if constexpr (i1 - i0 == 2) sb[(i0 + 1) >> 2][(i0 + 1) & 3] = rdq(4, (i0 + 1) >> 2, (i0 + 1) & 3); }
        SB();
        mm(std::integral_constant<int, G>{});
      });
      CYC(cy[3]);
#pragma unroll
      for (int t = 0; t < 3; ++t) { unsigned d = fold_pack(acc[t]); asm volatile("" : "+v"(d));
        *(LAS unsigned short*)(imw + t * 256) = (unsigned short)d; *(LAS unsigned short*)(imw + t * 256 + 16) = (unsigned short)(d >> 16); SB(); }
#pragma unroll
      for (int t = 0; t < 6; ++t) { ac2[t] = (f4){0, 0, 0, 0}; asm volatile("" : "+v"(ac2[t])); }
    }
    bar();
    CYC(cy[4]);
    {
      const v4u a0 = lds16(imr), a1 = lds16(imr + 1024), a2 = lds16(imr + 2048);
      SB();
#pragma unroll
      for (int t = 6; t < 10; ++t) { ac2[t] = (f4){0, 0, 0, 0}; asm volatile("" : "+v"(ac2[t])); }
      float fa = 0.f, fb = 0.f; unsigned f[12];
      f[0] = f[1] = 0u;
      asm volatile("" : "+v"(f[0]), "+v"(f[1]));
      SB();
      auto sw32 = [&](int i, int j2) { auto r = __builtin_amdgcn_permlane32_swap(f[i], f[j2], false, false); f[i] = r[0]; f[j2] = r[1]; asm volatile("" : "+v"(f[i]), "+v"(f[j2])); };
      auto sw16 = [&](int i, int j2) { auto r = __builtin_amdgcn_permlane16_swap(f[i], f[j2], false, false); f[i] = r[0]; f[j2] = r[1]; asm volatile("" : "+v"(f[i]), "+v"(f[j2])); };
      static_for<41>([&](auto GC) {
        constexpr int G = decltype(GC)::value, t = 2 * (G / 6) + (G & 1), k = (G % 6) >> 1;
        if constexpr (G < 30) ac2[t] = smfmac64(k == 0 ? a0 : k == 1 ? a1 : a2, w2[t < 10 ? t : 0][k], ac2[t], sidx);
        SB();
        if constexpr (G >= 7 && G < 37) {
          constexpr int tp = (G - 7) / 3, ph = (G - 7) % 3;
          if constexpr (ph == 0) { fa = ac2[tp][0] + ac2[tp][2]; asm volatile("" : "+v"(fa)); }
          if constexpr (ph == 1) { fb = ac2[tp][1] + ac2[tp][3]; asm volatile("" : "+v"(fb)); }
          if constexpr (ph == 2) { f[2 + tp] = pk2(fa, fb); asm volatile("" : "+v"(f[2 + tp])); }
        }
        if constexpr (G >= 13 && G % 6 == 1) sw16(2 * ((G - 7) / 6), 2 * ((G - 7) / 6) + 1);
        if constexpr (G >= 14 && G % 12 == 2) sw32(4 * (G / 12 - 1), 4 * (G / 12 - 1) + 2);
        if constexpr (G >= 15 && G % 12 == 3) sw32(4 * (G / 12 - 1) + 1, 4 * (G / 12 - 1) + 3);
        if constexpr (G >= 16 && G % 12 == 4) { constexpr int q = G / 12 - 1;
          st16c(rw, q == 0 ? umo2 : umo, ub + (unsigned)q * 65536u, (v4u){f[4 * q], f[4 * q + 1], f[4 * q + 2], f[4 * q + 3]}); }
        SB();
      });
    }
    if (w == 3 && lane == 0) report(rw, 32 + h, s, cclk() - c0 - 150u, pol >> 3);
    CYC(cy[5]);
    if (w == 3) { STAMP(32 + h, 16 + s * 8 + 3); STAMPV(32 + h, 16 + s * 8 + 7, first ? 0 : 1); }
    if (w == 0 && s + 1 < NSEG) {
      const unsigned e = wait_word(rw, hb + ho_phys(HO_E), dead);
      if (lane == 0) lds4w(V_T, e);
    }
    bar();
    { const unsigned e = __builtin_amdgcn_readfirstlane(lds4(V_T)); tpass = (e >> 8) + (e & 255u); }
  }
}

constexpr unsigned R_P = 0, R_E = 2048, R_C = 2064;
__device__ __forceinline__ void role_r(const Params& p, unsigned char* ws, rsrc_t rw, int c, int lane, int w) {
  const int g = lane >> 4, n = lane & 15;
  bool dead = false;
  const unsigned pol = stripe_word(rw, lane); unsigned hprev = 0;
  wait_ge(rw, C_SET1, NSETUP - 1, dead);
  const unsigned p0 = pol >> 3;
  const int sidx = sparse_idx(lane);
  const unsigned l16 = lane * 16u;
  const int fi = 2 * (lane >> 4) + ((lane >> 2) & 1), fq = 2 * (lane & 3) + ((lane >> 3) & 1);
  const unsigned so = g * 256 + n * 8;
  const unsigned prd = opaque(R_P + (fi & 1) * 256 + ((fi >> 1) * 16 + 2 * fq) * 4), pwr = opaque(R_P + w * 512 + lane * 4);
  const unsigned mo = lane * 4;
  const unsigned cadr = opaque(R_C + (lane ? 4 + (lane & 31) * 4 : 0)), cinc = opaque(lane ? 0u : 1u), crd = opaque(R_C);
  unsigned tbulk = rclk();
  Hint uxf(C_UFLAG + w * 8, 8, lane);
  if (w == 0 && lane == 0) lds4w(R_C, 0u);
  for (int s = 0; s < NSEG; ++s) {
    const unsigned hb = ho_base(s, pol);
    const unsigned umb = WS_UM + (unsigned)((s & 1) * 40 + c) * 16384u + (((pol >> 2) & 1) << 13);
    const bool lastseg = s + 1 == NSEG;
    uxf.need(rw, s, dead);
    wait_until(tbulk);
    unsigned cb0 = 0, cb1 = 0, cb2 = 0; (void)cb0; (void)cb1; (void)cb2;
    CYC(cb0);
    v8u ux[4];
#pragma unroll
    for (int k = 0; k < 4; ++k) {
      const unsigned o = ux_at(s, c, w * 8, p0) + 2 * k * 1024;
      const v4u lo = ld16c(rw, l16, o), hi = ld16c(rw, l16, o + 1024);
      ux[k] = (v8u){lo.x, lo.y, lo.z, lo.w, hi.x, hi.y, hi.z, hi.w};
    }
    if (!uxf.has(s + 1)) uxf.sample(rw);
    if (w == 0 && !lastseg) { wait_until(tbulk + 28); const unsigned e0 = wait_word(rw, hb + ho_phys(HO_E), dead); if (lane == 0) lds4w(R_E, e0); }
    VMWAIT(0);
    CYC(cb1);
    bar();
    CYC(cb2);
    const unsigned ew = __builtin_amdgcn_readfirstlane(lds4(R_E)), e = ew >> 8;
    unsigned tk1 = 0; (void)tk1;
    const unsigned pbase = lastseg ? hprev : hb;
    const unsigned pm = sopaque(pbase + ho_phys(HO_P + w * 1024)), pxo = sopaque(pbase + ho_phys(HO_P + 4096 + w * 4096));
    const unsigned hn = sopaque(ho_base(s + 1, pol) + ho_phys(HO_MEM + c * 256));
    hprev = hb; tbulk = e + (ew & 255u) + PACE_UM;
    const unsigned umw = sopaque(umb + w * 2048);
    const unsigned ut = sopaque(umt_at(s, c, p0) + w * 2048);
    const unsigned full = sopaque(lastseg ? 0u : 4u * (unsigned)s + 4u);
    v8u u4; unsigned c0, sp = 0, why = 0, cy[8] = {0, 0, 0, 0, 0, 0, 0, 0}; (void)cy;
    {
      v4u pf[5]; f4 d;
      auto mul = [&]() -> bool {
#pragma unroll
        for (int f = 0; f < 4; ++f) { pf[f] = ld16c(rw, l16 + f * 1024, pxo); SB(); }
        const v2u x0 = __builtin_amdgcn_raw_buffer_load_b64(rw, so, pm, 16), x1 = __builtin_amdgcn_raw_buffer_load_b64(rw, so + 128, pm, 16);
        pf[4] = (v4u){x0.x, x0.y, x1.x, x1.y};
        SB();
        const v4u lo = ld16c(rw, l16, umw), hi = ld16c(rw, l16 + 1024, umw);
        u4 = (v8u){lo.x, lo.y, lo.z, lo.w, hi.x, hi.y, hi.z, hi.w};
        SB();
        d = (f4){0, 0, 0, 0};
        asm volatile("" : "+v"(d));
        SB();
#pragma unroll
        for (int f = 0; f < 4; ++f) d = smfmac64(pf[f], ux[f], d, sidx);
        d = smfmac64(pf[4], u4, d, sidx);
        CYC(cy[3]);
        const float s0 = d[0] + d[2], s1 = d[1] + d[3];
        lds4w(pwr, f2u(s0)); lds4w(pwr + 256, f2u(s1));
        SB();
        return __any(__builtin_isunordered(s0, s1));
      };
      if (!lastseg) wait_until(e);
      c0 = cclk();
      CYC(cy[0]);
      SB();
      if (__builtin_expect(mul(), 0)) {
        for (;; ++sp) {
          unsigned m = 0;
#pragma unroll
          for (int f = 0; f < 5; ++f) m |= or4(pf[f]);
          const unsigned m2 = max(max(max(u4[0], u4[1]), max(u4[2], u4[3])), max(max(u4[4], u4[5]), max(u4[6], u4[7])));
          if (sp == 0) why = (__all((m & 0x80008000u) == 0) ? 0 : 1) + (__all(m2 != SENT) ? 0 : 256);
          if (__all((m & 0x80008000u) == 0 && m2 != SENT) || dead) break;
          if (sp > SPINCAP) { dead = true; st4c(rw, 0, WS_CTRL + C_TMO * 256, 3000 + s); }
          asm volatile("" ::: "memory");
          mul();
        }
      }
    }
    CYC(cy[4]);
    {
      unsigned cnt; f2 t0, t1, t2, t3;
      asm volatile("ds_add_u32 %5, %6\n\tds_read_b32 %0, %8\n\t"
                   "ds_read_b64 %1, %7\n\tds_read_b64 %2, %7 offset:512\n\tds_read_b64 %3, %7 offset:1024\n\tds_read_b64 %4, %7 offset:1536\n\t"
                   "s_waitcnt lgkmcnt(0)"
                   : "=&v"(cnt), "=&v"(t0), "=&v"(t1), "=&v"(t2), "=&v"(t3) : "v"(cadr), "v"(cinc), "v"(prd), "v"(crd) : "memory");
      const f2 t = (t0 + t1) + (t2 + t3);
      st4c(rw, cnt == full ? mo : 0xF0000000u, hn, pk2(t[0], t[1]));
      SB();
      CYC(cy[6]);
    }
    bar();
    if (w == 0 && lane == 0) report(rw, 64 + c, s, cclk() - c0, p0);
    (void)why;
    STAMPV(64 + c, 16 + s * 8 + 4 + w, why | ((cy[3] - cy[0]) << 16));
    if (w == 2 && s < 40) { STAMPV(64 + c, 860 + s * 4, cb1 - cb0); STAMPV(64 + c, 861 + s * 4, cb2 - cb0); }
    st16c(rw, l16, ut, (v4u){u4[0], u4[1], u4[2], u4[3]}); st16c(rw, l16, ut + 1024, (v4u){u4[4], u4[5], u4[6], u4[7]});
    st16c(rw, l16, umw, (v4u){SENT, SENT, SENT, SENT}); st16c(rw, l16 + 1024, umw, (v4u){SENT, SENT, SENT, SENT});
    unsigned cb3 = 0, cb4 = 0; (void)cb3; (void)cb4;
    CYC(cb3);
    VMWAIT(0);
    bar();
    CYC(cb4);
    if (w == 2 && s < 40) STAMPV(64 + c, 862 + s * 4, cb4 - cb3);
    if (w == 3 && lane == 0) st4c(rw, 0, stripe(WS_CTRL + 65536, c * 128u, p0), (unsigned)s);
  }
}

__device__ __forceinline__ void role_t(const Params& p, unsigned char* ws, rsrc_t rw, int t, int lane, int w) {
  const int g = lane >> 4, n = lane & 15;
  bool dead = false;
  rsrc_t rx = mk_rsrc(p.x, S_ * D_ * 2);
  const unsigned l16 = lane * 16u, vm0 = g < 2 ? l16 : 0xF0000000u;
  const int cf = t + 20 * (w >> 1);
  unsigned p0 = 0;
  struct TB { v4u ux[16]; v2u xv; };
  TB A, B; v4u um[4], px[20];
  p0 = stripe_word(rw, lane) >> 3;
  const unsigned fo = (lane & 15) < 8 ? (C_UFLAG + w * 8 + (lane & 7)) * 256u : stripe(WS_CTRL + 65536, (t + 20 * (lane & 1)) * 128u, p0) - WS_CTRL;
  int fl = -1;
  auto need = [&](int s, int sr) {
    for (unsigned sp = 0; !__all(fl >= ((lane & 8) ? sr : s)) && !dead; ++sp) {
      asm volatile("" ::: "memory");
      if (sp) __builtin_amdgcn_s_sleep(2);
      fl = (int)ld4c(rw, fo, WS_CTRL);
      if (sp > SPINCAP) { dead = true; st4c(rw, 0, WS_CTRL + C_TMO * 256, 4500 + s); }
    }
    __builtin_amdgcn_fence(__ATOMIC_ACQUIRE, "wavefront");
  };
  auto ld_px = [&](int s, int i) {
    px[i] = i < 16 ? ld16c(rw, (i & 1) ? l16 : vm0, px_at(s, w * 16 + (i & 8), p0) + (i & 7) * 1024) : ld16c(rw, l16, px_at(s, 64 + w * 4, p0) + (i - 16) * 1024);
  };
  auto ld_early = [&](TB& b, int s, int k, unsigned vo) {
    if (k < 8) { b.ux[2 * (k < 8 ? k : 0)] = ld16c(rw, vo, ux_at(s, t, w * 8, p0) + k * 1024); b.ux[2 * (k < 8 ? k : 0) + 1] = ld16c(rw, vo, ux_at(s, t + 20, w * 8, p0) + k * 1024); }
    if (k == 8) b.xv = __builtin_amdgcn_raw_buffer_load_b64(rx, (unsigned)((s * 32 + (w & 1) * 16 + n) * D_ + 16 * cf + 4 * g) * 2, 0, 0);
  };
  const unsigned ho = (unsigned)(((2 * (cf & 1) + (g >> 1)) * 16 + n) * 16 + (g & 1) * 8), hb0 = WS_HF + (unsigned)((w & 1) * 20 + (cf >> 1)) * 1024u;
  for (int s = 0; s < ARM; ++s) st8c(rw, ho, hb0 + (unsigned)s * 40960u, (v2u){SENT, SENT});
  need(0, -1);
#pragma unroll
  for (int k = 0; k < 9; ++k) ld_early(A, 0, k, l16);
  auto seg = [&](int s, TB& cur, TB& nxt) {
    const int sn = s + 1 < NSEG ? s + 1 : s;
    unsigned cy[8] = {0, 0, 0, 0, 0, 0, 0, 0}; (void)cy;
    CYC(cy[0]);
    VMWAIT(0);
    need(sn, s);
    CYC(cy[1]);
#pragma unroll
    for (int i = 0; i < 20; ++i) ld_px(s, i);
#pragma unroll
    for (int i = 0; i < 4; ++i) um[i] = ld16c(rw, l16, umt_at(s, t + 20 * (i & 1), p0) + (w * 2 + (i >> 1)) * 1024);
    SB();
    const unsigned red = opaque((s & 1) * 16384 + l16), le = s + 1 < NSEG ? l16 : 0xF0000000u;
    f4 acc[4];
    auto mul = [&](auto PRE) {
#pragma unroll
      for (int i = 0; i < 4; ++i) acc[i] = (f4){0, 0, 0, 0};
#pragma unroll
      for (int k = 0; k < 10; ++k) {
#pragma unroll
        for (int i = 0; i < 4; ++i) acc[i] = mfma32(k < 8 ? cur.ux[k < 8 ? 2 * k + (i >> 1) : 0] : um[k < 8 ? 0 : 2 * (k - 8) + (i >> 1)], px[2 * k + (i & 1)], acc[i]);
        if (decltype(PRE)::value && k < 4) ld_early(nxt, sn, k, le);
        SB();
      }
    };
    mul(std::true_type{});
    CYC(cy[2]);
    if (__builtin_expect(__any(__builtin_isunordered(acc[0][0], acc[1][0])), 0)) {
      for (unsigned sp = 0;; ++sp) {
        asm volatile("" ::: "memory");
        unsigned m = 0;
#pragma unroll
        for (int i = 0; i < 20; ++i) ld_px(s, i);
#pragma unroll
        for (int i = 0; i < 20; ++i) m |= or4(px[i]);
        if (__all((m & 0x80008000u) == 0) || dead) break;
        if (sp > SPINCAP) { dead = true; st4c(rw, 0, WS_CTRL + C_TMO * 256, 4000 + s); }
      }
      mul(std::false_type{});
    }
#pragma unroll
    for (int i = 0; i < 4; ++i) lds16w(red + (w * 4 + i) * 1024, __builtin_bit_cast(v4u, acc[i]));
    SB();
    fl = (int)ld4c(rw, fo, WS_CTRL);
    CYC(cy[3]);
    bar();
    CYC(cy[4]);
    const unsigned rr = red + w * 1024;
    const f4 o = (__builtin_bit_cast(f4, lds16(rr)) + __builtin_bit_cast(f4, lds16(rr + 4096))) + (__builtin_bit_cast(f4, lds16(rr + 8192)) + __builtin_bit_cast(f4, lds16(rr + 12288)));
    const v2u xv = cur.xv;
    const v2u hv = {nosent(pk2(bflo(xv.x) + rbf(o[0]), bfhi(xv.x) + rbf(o[1]))), nosent(pk2(bflo(xv.y) + rbf(o[2]), bfhi(xv.y) + rbf(o[3])))};
    st8c(rw, ho, hb0 + (unsigned)s * 40960u, hv);
    if (w == 3 && lane == 0) st4c(rw, 0, WS_CTRL + (C_TFLAG + t) * 256, (unsigned)s);
    if (s + ARM < NSEG) st8c(rw, ho, hb0 + (unsigned)(s + ARM) * 40960u, (v2u){SENT, SENT});
    CYC(cy[5]);
    if (w == 0) STAMP(104 + t, 16 + s);
    if (w == 0 && t == 0) MARK(0, s);
    SB();
#pragma unroll
    for (int k = 4; k < 9; ++k) ld_early(nxt, sn, k, le);
    CYC(cy[6]);
  };
  for (int s = 0; s < NSEG; s += 2) { seg(s, A, B); seg(s + 1, B, A); }
}

__device__ __forceinline__ v2u ld8c(rsrc_t r, unsigned v, unsigned s) { return __builtin_amdgcn_raw_buffer_load_b64(r, v, s, 16); }

template <int NS, int NK>
__device__ __forceinline__ void load_wmix(const unsigned short* W, int N, int row0, int sl, int lane, int w, v4u (&wt)[NS][NK]) {
  constexpr int NB = NK / 4;
  const int gg = lane >> 4, hi = lane >> 5, r = (lane >> 2) & 3, cc = lane & 3;
  const int sl0 = min(sl, N / 32 - 2);
  const unsigned short* src = W + (size_t)(row0 + (lane >> 3)) * N + sl0 * 32 + (lane & 7) * 8;
  const unsigned lb = w * 40960;
  auto issue = [&](int blk) {
#pragma unroll
    for (int i = 0; i < 8; ++i) glds16(src + (size_t)(blk * 64 + i * 8) * N, lb + (blk % 5) * 8192 + i * 1024);
  };
#pragma unroll
  for (int b = 0; b < 5; ++b) issue(b);
  const unsigned base = opaque(lb + (8 * hi + r) * 128 + (sl - sl0) * 64 + (gg & 1) * 32 + (2 * (cc & 1) + (cc >> 1)) * 8);
  static_for<NB>([&](auto BC) {
    constexpr int b = decltype(BC)::value, left = NB - 1 - b < 4 ? NB - 1 - b : 4;
    if constexpr (left == 4) VMWAIT(32); else if constexpr (left == 3) VMWAIT(24); else if constexpr (left == 2) VMWAIT(16); else if constexpr (left == 1) VMWAIT(8); else VMWAIT(0);
#pragma unroll
    for (int i = 0; i < NS; ++i)
#pragma unroll
      for (int k = 0; k < 4; ++k) { const unsigned a = base + (b % 5) * 8192 + k * 2048 + i * 64; v2u lo = trread(a), hi2 = trread(a + 512); wt[i][4 * b + k] = (v4u){lo.x, lo.y, hi2.x, hi2.y}; }
    asm volatile("s_waitcnt lgkmcnt(0)" ::: "memory");
    if constexpr (b + 5 < NB) issue(b + 5);
  });
#pragma unroll
  for (int i = 0; i < NS; ++i)
#pragma unroll
    for (int k = 0; k < NK; ++k) if (k * NS < 64) PIN_A(wt[i][k]);
}

constexpr unsigned M_ST = 81920, M_CS = M_ST + 2048;
template <int STAGE, int NS, int NPROD, bool JIT>
__device__ __forceinline__ void role_mix(const Params& p, unsigned char* ws, rsrc_t rw, int j, int S0, int S1, unsigned MYFLAG, unsigned INFLAG, int lane, int w) {
  int par = 1;
  constexpr int OKS = STAGE == 3 ? 48 : 20, NG = 40 * NS;
  constexpr unsigned IN = STAGE == 1 ? WS_HF : STAGE == 2 ? WS_H1F : WS_X1F, OUT = STAGE == 1 ? WS_H1F - 20480u : STAGE == 2 ? WS_X1F : WS_H2F;
  const int g = lane >> 4, n = lane & 15, hi = lane >> 5, tok = lane & 31;
  const unsigned l16 = opaque(lane * 16u), OOB = 0xF0000000u;
  bool dead = false;
  const int sl = (j * 4 + w) * NS;
  unsigned vo[NS];
#pragma unroll
  for (int i = 0; i < NS; ++i) {
    const int q = (sl + i) / 5, tt = STAGE == 1 ? tok + 16 + (q == 0 ? 1 : q == 1 ? -1 : q == 2 ? 2 : -2) : tok;
    vo[i] = (unsigned)((tt >> 4) * (OKS * 1024) + (sl + i) * 1024 + (hi * 16 + (tt & 15)) * 16);
  }
  auto put = [&](int s, int c, v4u o) { st16c(rw, vo[c >> 1], OUT + (unsigned)s * (OKS * 2048u) + (c & 1) * 512, o); };
  const bool stat_writer = STAGE == 1 && j == 0 && w == 0;
  const unsigned sto = hi == 0 ? tok * 8u : OOB;
  v4u wt[NS][40];
  load_wmix<NS, 40>(STAGE == 1 ? p.w1 : STAGE == 2 ? p.w2 : p.w3, STAGE == 3 ? F_ : D_, 0, sl, lane, w, wt);
  v4u ones = {0x3F803F80u, 0x3F803F80u, 0x3F803F80u, 0x3F803F80u};
  asm volatile("s_nop 1" : "+v"(ones));
  bar();
  const unsigned csa = opaque(M_CS + w * 512 + hi * 256);
  f4 cu[2] = {{0, 0, 0, 0}, {0, 0, 0, 0}};
  if (STAGE != 2) {
    f16v cs[NS];
    static_for<NG>([&](auto GC) { constexpr int G = decltype(GC)::value, k = G / NS, i = G % NS; mfma_big<k == 0, (k * NS < 64), false>(cs[i], wt[i][k], ones); });
    PAD_BIG();
#pragma unroll
    for (int q = 0; q < 4 * NS; ++q) { const f16v a = cs[q >> 2]; const int o = 4 * (q & 3); lds16w(csa + q * 16, (v4u){f2u(a[o]), f2u(a[o + 1]), f2u(a[o + 2]), f2u(a[o + 3])}); }
    cu[0] = __builtin_bit_cast(f4, lds16(csa)); cu[1] = __builtin_bit_cast(f4, lds16(csa + 16));
  }
  auto arm = [&](int s0, int c) {
    const int s = s0 < S1 ? s0 : NSEG + 2;
    st16c(rw, l16, OUT + (STAGE == 1 ? 20480u : 0u) + (unsigned)((s * 2 + (c & 1)) * OKS + sl + (c >> 1)) * 1024u, (v4u){SENT, SENT, SENT, SENT});
    if (stat_writer && c == 0) st8c(rw, sto, WS_ST + (unsigned)s * 256u, (v2u){SENT, SENT});
    if (STAGE == 2 && (c & 1)) st8c(rw, sto, WS_XST + (unsigned)(s * 20 + sl + (c >> 1)) * 256u, (v2u){SENT, SENT});
  };
  const int SA = STAGE == 1 ? S1 - 1 : S1;
  for (int s = S0; s < (JIT ? S0 + ARM + (STAGE == 1) : S1); ++s) {
#pragma unroll
    for (int c = 0; c < 2 * NS; ++c) arm(!JIT || s < S0 + ARM ? s : SA, c);
  }
  if (STAGE == 1) {
    VMWAIT(0);
#pragma unroll
    for (int c = 0; c < 2 * NS; ++c) { put(-1, c, (v4u){0u, 0u, 0u, 0u}); put(NSEG, c, (v4u){0u, 0u, 0u, 0u}); }
  }
  VMWAIT(0);
  bar();
  Hint hint(INFLAG, NPROD, lane);
  auto want = [&](int s) { return STAGE == 2 ? min(s + 1, NSEG - 1) : s; };
  f4 s1[2], s2[2];
  auto lda = [&](int s, int q) { return ld16c(rw, l16 + ((q >> 1) & 3) * 1024, IN + (unsigned)s * 40960u + w * 5120 + (q & 1) * 20480 + (q >> 3) * 4096); };
  unsigned inmax = 0; v2u xs[3];
  auto ldxs = [&](int s, int i) { xs[i] = ld8c(rw, i < 2 ? hi * 256u + tok * 8u : sto, WS_XST + (unsigned)(s * 20 + 5 * w + 2 * i) * 256u); };
  auto in_piece = [&](unsigned dst, int q, v4u a) {
    if (STAGE == 3) { inmax = max(q ? inmax : 0u, mx4(a)); asm volatile("" : "+v"(inmax)); }
    else if (q < 2) mfma_v0(s1[q & 1], ones, a); else mfma_v(s1[q & 1], ones, a);
    if (STAGE == 1) { if (q < 2) mfma_v0(s2[q & 1], a, a); else mfma_v(s2[q & 1], a, a); }
    lds16w(dst + q * 1024, a);
    if (STAGE != 3 && q == 9) asm volatile("s_nop 7");
    SB();
  };
  auto suspect = [&]() -> bool {
    if (STAGE == 3) return __any(max(max(inmax, max(xs[0].x, xs[0].y)), max(max(xs[1].x, xs[1].y), max(xs[2].x, xs[2].y))) == SENT);
    return __any(__builtin_isunordered(s1[0][0], s1[1][0]));
  };
  auto in_all = [&](int s, unsigned dst) -> bool {
    v4u a[10]; unsigned m = 0;
#pragma unroll
    for (int q = 0; q < 10; ++q) a[q] = lda(s, q);
    if (STAGE == 3) { ldxs(s, 0); ldxs(s, 1); ldxs(s, 2); }
#pragma unroll
    for (int q = 0; q < 10; ++q) in_piece(dst, q, a[q]);
    if (__builtin_expect(!suspect(), 1)) return true;
    if (STAGE == 3) return false;
#pragma unroll
    for (int q = 0; q < 10; ++q) m = max(m, mx4(a[q]));
    return __all(m != SENT);
  };
  auto in_finish = [&](int s, unsigned dst, bool again) {
    if (again) {
      for (unsigned sp = 0;; ++sp) {
        asm volatile("" ::: "memory");
        if (in_all(s, dst) || dead) break;
        if (sp > SPINCAP) { dead = true; st4c(rw, 0, WS_CTRL + C_TMO * 256, 7100 + s); }
      }
    }
    if (STAGE == 1) {
      const float d0 = (n & 2) ? ((n & 1) ? s2[0][3] : s2[0][2]) : ((n & 1) ? s2[0][1] : s2[0][0]);
      const float d1 = (n & 2) ? ((n & 1) ? s2[1][3] : s2[1][2]) : ((n & 1) ? s2[1][1] : s2[1][0]);
      if (g == (n >> 2)) lds16w(M_ST + (par ^ 1) * 1024 + w * 256 + n * 16, (v4u){f2u(s1[0][0]), f2u(d0), f2u(s1[1][0]), f2u(d1)});
    }
    if (STAGE == 3) {
      const float a = xor32_add(u2f(xs[0].x) + u2f(xs[1].x) + u2f(xs[2].x)), b = xor32_add(u2f(xs[0].y) + u2f(xs[1].y) + u2f(xs[2].y));
      *(LAS f2*)(M_ST + (par ^ 1) * 1024 + w * 256 + n * 16 + (g & 1) * 8) = (f2){a, b};
    }
  };
  const unsigned xo = (g & 1) * 1024 + hi * 256 + n * 16;
  auto xoff = [](int k) { return (k >> 1) * 2048 + (k & 1) * 512; };
  v4u xr[4]; f2 sp4[4];
  auto head = [&]() {
    par ^= 1;
    const unsigned src = par * 40960 + xo;
    xr[0] = lds16(src); xr[1] = lds16(src + xoff(1)); xr[2] = lds16(src + xoff(2));
    if (STAGE != 2) {
#pragma unroll
      for (int q = 0; q < 4; ++q) sp4[q] = *(LAS f2*)(M_ST + par * 1024 + q * 256 + n * 16 + (g & 1) * 8);
    }
    SB();
  };
  auto fetch = [&](int s) {
    hint.need(rw, want(s), dead);
    const unsigned dst = opaque((par ^ 1) * 40960 + w * 10240 + l16);
    in_finish(s, dst, true);
    bar();
    head();
  };
  float pv[16 * NS]; v4u phv[2 * NS]; float prs = 0.f, pnm = 0.f, pga = 0.f, pgb = 0.f, xa = 0.f, xb = 0.f, u[8]; v4u po;
#pragma unroll
  for (int i = 0; i < NS; ++i) { phv[2 * i] = phv[2 * i + 1] = ones; for (int e = 0; e < 16; ++e) pv[16 * i + e] = 0.f; }
  auto epi_two = [&](auto PC, float* y, int c, int e0) {
    constexpr int ph = decltype(PC)::value;
#pragma unroll
    for (int e = e0; e < e0 + 2; ++e) {
      if constexpr (STAGE != 2) {
        if constexpr (ph == 0) y[e] = __builtin_fmaf(pnm, cu[e >> 2][e & 3], y[e]);
        if constexpr (ph == 1) u[e] = y[e] * y[e];
        if constexpr (ph == 2) u[e] = __builtin_fmaf(u[e], pga, pgb);
        if constexpr (ph == 3) u[e] *= y[e];
        if constexpr (ph == 4) u[e] = __builtin_amdgcn_exp2f(u[e]);
        if constexpr (ph == 5) u[e] = __builtin_fmaf(u[e], prs, prs);
        if constexpr (ph == 6) u[e] = __builtin_amdgcn_rcpf(u[e]);
        if constexpr (ph == 7) y[e] *= u[e];
      } else {
        if constexpr (ph == 0) u[e] = (e & 1) ? bfhi(phv[c][e >> 1]) : bflo(phv[c][e >> 1]);
        if constexpr (ph == 1) u[e] = __builtin_fmaf(u[e], prs, pnm);
        if constexpr (ph == 2) y[e] += u[e];
        if constexpr (ph == 3) { xa = ((c & 1) || e) ? xa + y[e] : y[e]; xb = ((c & 1) || e) ? __builtin_fmaf(y[e], y[e], xb) : y[e] * y[e]; }
      }
      asm volatile("" : "+v"(u[e]), "+v"(y[e]));
    }
  };
  auto epi_step = [&](auto EC, int s) {
    constexpr int E = decltype(EC)::value, c = E / 20, gi = E % 20;
    float* y = pv + 8 * c;
    if constexpr (gi < 16) epi_two(std::integral_constant<int, (gi >> 1)>{}, y, c, 2 * (gi & 1));
    if constexpr (gi >= 2 && gi < 18) epi_two(std::integral_constant<int, ((gi - 2) >> 1)>{}, y, c, 4 + 2 * (gi & 1));
    if constexpr (STAGE != 2 && (gi == 16 || gi == 17)) cu[gi & 1] = __builtin_bit_cast(f4, lds16(csa + ((c + 1) % (2 * NS)) * 32 + (gi & 1) * 16));
    if constexpr (STAGE == 2 && (c & 1) && gi == 12) st8c(rw, sto, WS_XST + (unsigned)(s * 20 + sl + (c >> 1)) * 256u, (v2u){nosent(f2u(xor32_add(xa))), nosent(f2u(xor32_add(xb)))});
    if constexpr (gi == 18) { po = (v4u){pk2(y[0], y[1]), pk2(y[2], y[3]), pk2(y[4], y[5]), pk2(y[6], y[7])}; asm volatile("" : "+v"(po)); }
    if constexpr (gi == 19) {
      put(s, c, STAGE == 2 ? (v4u){nosent(po.x), nosent(po.y), nosent(po.z), nosent(po.w)} : po);
      if constexpr (E == NG - 1) {
        if (w == 3 && lane == 0) st4c(rw, 0, WS_CTRL + (MYFLAG + j) * 256, (unsigned)s);
        if (w == 0) STAMP(232 + (STAGE - 1) * 4 + (j & 3), 16 + s);
        if (w == 0 && j == (STAGE == 1 ? 4 : 0)) MARK(STAGE, s);
      }
    }
  };
#pragma unroll
  for (int e = 0; e < 8; ++e) u[e] = 0.f;
  bool pending = false;
  fetch(S0);
  for (int s = S0; s < S1; ++s) {
    const int sn = s + 1;
    unsigned cy[8] = {0, 0, 0, 0, 0, 0, 0, 0}; (void)cy;
    CYC(cy[0]);
    const bool ready = NS == 1 ? (sn + 1 < S1 && hint.has(want(sn) + 1)) : (sn < S1 && hint.has(want(sn)));
    __builtin_amdgcn_fence(__ATOMIC_ACQUIRE, "wavefront");
    const unsigned src = opaque(par * 40960 + xo), dst = opaque((par ^ 1) * 40960 + w * 10240 + l16);
    f16v acc[NS]; v4u hv[2 * NS]; v2u stv = {0, 0}; float mu = 0.f, rstd = 0.f;
    if (STAGE != 2) {
      const f2 st = (sp4[0] + sp4[1]) + (sp4[2] + sp4[3]);
      mu = st[0] * (1.f / D_);
      const float var = fmaxf(st[1] * (1.f / D_) - mu * mu, 0.f);
      rstd = __builtin_amdgcn_rsqf(var + 1e-5f);
      if (stat_writer) st8c(rw, sto, WS_ST + (unsigned)s * 256u, (v2u){nosent(f2u(mu)), nosent(f2u(rstd))});
      if (STAGE == 1) rstd *= __builtin_amdgcn_rsqf(var * rstd * rstd + 1e-5f);
    }
    SB();
    auto body = [&](auto PRE, auto EPI) {
      constexpr int AB = NS == 1 ? 10 : 5, L0 = 0, LS = NS == 1 ? 1 : 2, WS0 = NS == 1 ? 18 : 10, WSS = 2;
      v4u an[AB];
      static_for<NG>([&](auto GC) {
        constexpr int G = decltype(GC)::value, k = G / NS, i = G % NS;
        mfma_big<k == 0, (k * NS < 64), false>(acc[i], wt[i][k], xr[k & 3]);
        SB();
        if constexpr (i == 0 && k < 37) xr[(k + 3) & 3] = lds16(src + xoff(k + 3));
        if constexpr (i == NS - 1) {
          if constexpr (JIT && k == 40 - 2 * NS) VMWAIT(0);
          if constexpr (JIT && k >= 40 - 2 * NS) arm(s + ARM < SA ? s + ARM : S1, k - (40 - 2 * NS));
          if constexpr (decltype(PRE)::value && STAGE == 3 && k == 33) asm volatile("" : "+v"(xs[0]), "+v"(xs[1]), "+v"(xs[2]));
          if constexpr (STAGE == 2 && k == 36) asm volatile("" : "+v"(stv), "+v"(hv[0]), "+v"(hv[2 * NS - 1]));
          if constexpr (JIT && k == 39 - 2 * NS) asm volatile("" : "+v"(hint.v));
          if constexpr (decltype(PRE)::value && k >= L0 && (k - L0) % LS == 0 && (k - L0) / LS < 10) an[((k - L0) / LS) % AB] = lda(sn, (k - L0) / LS);
          if constexpr (k == (NS == 1 ? 16 : 25)) hint.sample(rw);
          if constexpr (decltype(PRE)::value && STAGE == 3 && (k == 17 || k == 21 || k == 23)) ldxs(sn, k == 17 ? 0 : k == 21 ? 1 : 2);
          if constexpr (STAGE == 2 && k == 11) stv = ld8c(rw, tok * 8u, WS_ST + (unsigned)s * 256u);
          if constexpr (STAGE == 2 && k >= 12 && k < 12 + 2 * NS) hv[k - 12] = ld16c(rw, vo[(k - 12) >> 1], WS_HF + (unsigned)s * 40960u + ((k - 12) & 1) * 512);
        }
        if constexpr (decltype(EPI)::value) epi_step(GC, s - 1);
        SB();
        if constexpr (decltype(PRE)::value && i == 0 && k >= WS0 && (k - WS0) % WSS == 0 && (k - WS0) / WSS < 10) in_piece(dst, (k - WS0) / WSS, an[((k - WS0) / WSS) % AB]);
      });
      PAD_BIG();
      if (STAGE == 2) {
        for (unsigned sp = 0; __any(max(stv.x, stv.y) == SENT) && !dead; ++sp) {
          asm volatile("" ::: "memory");
          stv = ld8c(rw, tok * 8u, WS_ST + (unsigned)s * 256u);
          if (sp > SPINCAP) { dead = true; st4c(rw, 0, WS_CTRL + C_TMO * 256, 7200 + s); }
        }
        mu = u2f(stv.x); rstd = u2f(stv.y);
#pragma unroll
        for (int c = 0; c < 2 * NS; ++c) phv[c] = hv[c];
      }
#pragma unroll
      for (int e = 0; e < 16 * NS; ++e) pv[e] = acc[e >> 4][e & 15];
      if (STAGE == 2) { prs = rstd; pnm = -mu * rstd; }
      else {
        if (__builtin_isunordered(mu, rstd)) mu = rstd = u2f(0x7FC00000u);
        pnm = -mu; prs = __builtin_amdgcn_rcpf(rstd);
        pgb = -2.302208198144325f * rstd; pga = 0.044715f * rstd * rstd * pgb;
      }
    };
    if (ready) {
      CYC(cy[2]);
      if (pending) body(std::true_type{}, std::true_type{}); else body(std::true_type{}, std::false_type{});
      CYC(cy[3]);
      in_finish(sn, dst, __builtin_expect(suspect(), 0));
      bar();
      head();
      CYC(cy[4]);
      pending = true;
    } else {
      if (pending) body(std::false_type{}, std::true_type{}); else body(std::false_type{}, std::false_type{});
      pending = false;
      if (NS == 1 && sn < S1 && hint.has(want(sn))) {
        __builtin_amdgcn_fence(__ATOMIC_ACQUIRE, "wavefront");
        v4u a[10];
#pragma unroll
        for (int q = 0; q < 10; ++q) a[q] = lda(sn, q);
        if (STAGE == 3) { ldxs(sn, 0); ldxs(sn, 1); ldxs(sn, 2); }
        SB();
        static_for<NG>([&](auto EC) { epi_step(EC, s); SB(); });
#pragma unroll
        for (int q = 0; q < 10; ++q) in_piece(dst, q, a[q]);
        in_finish(sn, dst, __builtin_expect(suspect(), 0));
        bar();
        head();
      } else {
        static_for<NG>([&](auto EC) { epi_step(EC, s); SB(); });
        if (sn == S1) break;
        fetch(sn);
#pragma unroll
        for (int e = 0; e < 16 * NS; ++e) pv[e] = 0.f;
      }
    }
  }
}

__device__ __forceinline__ void role_mix4(const Params& p, unsigned char* ws, rsrc_t rw, int j, int S0, int S1, unsigned INFLAG, int NPROD, int lane, int w) {
  const int g = lane >> 4, n = lane & 15, hi = lane >> 5;
  const unsigned l16 = lane * 16u;
  bool dead = false;
  v4u wt[2][24];
  load_wmix<2, 24>(p.w4, D_, 384 * w, 2 * j, lane, w, wt);
  bar();
  Hint hint(INFLAG, NPROD, lane);
  const int slab = 2 * j + (w >> 1);
  const unsigned ao = (g & 1) * 49152u + hi * 256 + n * 16, fo = (g & 1) * 20480u + slab * 1024 + (w & 1) * 512 + hi * 256 + n * 16;
  v4u a[24];
  auto lda = [&](int s, int k, unsigned vo) { a[k] = ld16c(rw, vo, WS_H2F + (unsigned)(s * 96 + 12 * w + (k >> 1)) * 1024u + (k & 1) * 512); };
  hint.need(rw, S0, dead);
#pragma unroll
  for (int q = 0; q < 24; ++q) lda(S0, q, ao);
  for (int s = S0; s < S1; ++s) {
    const int sn = s + 1 < S1 ? s + 1 : s;
    hint.need(rw, sn, dead);
    const unsigned aon = s + 1 < S1 ? ao : 0xF0000000u;
    const v4u hv = ld16c(rw, fo, WS_HF + (unsigned)s * 40960u), xv = ld16c(rw, fo, WS_X1F + (unsigned)s * 40960u);
    f16v acc[2];
    auto body = [&]() {
      static_for<24>([&](auto KC) {
        constexpr int k = decltype(KC)::value;
        mfma_big<k == 0, true, false>(acc[0], wt[0][k], a[k]);
        if constexpr (k == 8) hint.sample(rw);
        SB();
        mfma_big<k == 0, true, false>(acc[1], wt[1][k], a[k]);
        SB();
        lda(sn, k, aon);
        SB();
      });
      PAD_BIG();
    };
    body();
    if (__builtin_expect(__any(acc[0][0] != acc[0][0]), 0)) {
      for (unsigned sp = 0;; ++sp) {
        asm volatile("" ::: "memory");
        unsigned m = 0;
#pragma unroll
        for (int q = 0; q < 24; ++q) lda(s, q, ao);
#pragma unroll
        for (int q = 0; q < 24; ++q) m = max(m, mx4(a[q]));
        if (__all(m != SENT) || dead) break;
        if (sp > SPINCAP) { dead = true; st4c(rw, 0, WS_CTRL + C_TMO * 256, 7300 + s); }
      }
      body();
    }
    const unsigned red = opaque((s & 1) * 32768 + l16);
#pragma unroll
    for (int i = 0; i < 8; ++i) { const f16v d = acc[i >> 2]; const int o = 4 * (i & 3);
      lds16w(red + (w * 8 + i) * 1024, (v4u){f2u(d[o]), f2u(d[o + 1]), f2u(d[o + 2]), f2u(d[o + 3])}); }
    bar();
    f4 o[2];
#pragma unroll
    for (int tt = 0; tt < 2; ++tt) {
      const unsigned rr = red + (w * 2 + tt) * 1024;
      o[tt] = (__builtin_bit_cast(f4, lds16(rr)) + __builtin_bit_cast(f4, lds16(rr + 8192))) + (__builtin_bit_cast(f4, lds16(rr + 16384)) + __builtin_bit_cast(f4, lds16(rr + 24576)));
    }
    v4u r;
#pragma unroll
    for (int q = 0; q < 4; ++q)
      r[q] = pk2(bflo(hv[q]) + rbf(bflo(xv[q]) + rbf(o[q >> 1][2 * (q & 1)])), bfhi(hv[q]) + rbf(bfhi(xv[q]) + rbf(o[q >> 1][2 * (q & 1) + 1])));
    *(v4u*)(p.out + (size_t)(s * 32 + (lane & 31)) * D_ + slab * 32 + (w & 1) * 16 + hi * 8) = r;
    if (w == 0) STAMP(244 + (j & 3), 16 + s);
    if (w == 0 && j == 0) MARK(4, s);
  }
}

__global__ void __launch_bounds__(256, 1) k_main(Params p) {
  extern __shared__ __attribute__((aligned(16))) unsigned char lds_raw[];
  unsigned char* ws = ws_base(p.ws);
  rsrc_t rw = mk_rsrc(ws, WS_END);
  const int tid = threadIdx.x, w = __builtin_amdgcn_readfirstlane(tid >> 6);
  if (tid == 0) {
    const unsigned xcc = (unsigned)__builtin_amdgcn_s_getreg(20 | (3 << 11)) & 7u;
    unsigned v = 0;
    for (unsigned k = 0; k < 8; ++k) {
      const unsigned x = (xcc + k) & 7u;
      const unsigned rk = __hip_atomic_fetch_add((unsigned*)(ws + WS_CTRL + (C_TICKET + x) * 256), 1u, __ATOMIC_RELAXED, __HIP_MEMORY_SCOPE_AGENT) + 1u;
      if (rk < 32u) { v = rk * 8 + x; break; }
    }
    lds4w(0, v);
  }
  bar();
  const int v = __builtin_amdgcn_readfirstlane(lds4(0));
  bar();
  const int x = v & 7, idx = (v >> 3) * 4 + (x & 3);
  if (x < 4 ? idx >= 64 : idx >= 104) setup_job(p, ws, rw, x < 4 ? idx - 64 : idx - 40, lane_id(), w);
  int s1 = -1, s4 = -1, s40 = 0, s41 = S3_SPLIT, s4n = 6; unsigned s4f = C_S3FLAG;
  if (x < 4) {
    if (idx < 32) role_y(p, ws, rw, idx, lane_id(), w);
    else if (idx < 64) role_v(p, ws, rw, idx - 32, lane_id(), w);
    else if (idx < 104) role_r(p, ws, rw, idx - 64, lane_id(), w);
    else if (idx < 124) role_t(p, ws, rw, idx - 104, lane_id(), w);
    else if (idx == 124) {
      if (w > 0) return;
      const int lane = lane_id();
      const unsigned pol = stripe_word(rw, lane);
      bool dead = false;
      const unsigned t00 = rclk(), c00 = cclk();
      wait_ge(rw, C_SET1, NSETUP - 1, dead);
      unsigned t = t00, esty = 2900, estr = 1500, c1 = 140, c3 = 72, la = 0, lb = 0; (void)la; (void)lb;
      for (int s = 0; s + 1 < NSEG; ++s) {
        const unsigned hb = ho_base(s, pol);
        wait_until(t);
        unsigned e = t + c1;
        for (unsigned sp = 0;; ++sp) {
          asm volatile("" ::: "memory");
          unsigned m = 0;
#pragma unroll
          for (int k = 0; k < 10; ++k) m = max(m, mx4(ld16c(rw, lane * 16, hb + ho_phys(HO_MEM + k * 1024))));
          if (__all(m != SENT) || dead) { if (sp) e = rclk() + c1 - PACE_LATE; break; }
          if (sp > SPINCAP) { dead = true; st4c(rw, 0, WS_CTRL + C_TMO * 256, 6000 + s); }
        }
        if (lane == 0) st4c(rw, 0, hb + ho_phys(HO_E), (e << 8) | c3);
        STAMPV(124, 32 + s, c1 * 256 + c3);
        t = e + c3;
        if (s >= 2) {
          unsigned a = ld4c(rw, lane * 128, stripe(WS_REP, 0, pol >> 3)), b = ld4c(rw, lane * 128, stripe(WS_REP, 8192, pol >> 3));
          a = (a >> 16) == (unsigned)(s - 1) ? a & 65535u : 0u; b = ((b >> 16) == (unsigned)(s - 1) && lane < 40) ? b & 65535u : 0u;
#pragma unroll
          for (int o = 1; o < 64; o <<= 1) { a = max(a, (unsigned)__shfl_xor((int)a, o)); b = max(b, (unsigned)__shfl_xor((int)b, o)); }
          a = __builtin_amdgcn_readfirstlane(a); b = __builtin_amdgcn_readfirstlane(b);
          la = a; lb = b;
          if (a > 1000u && a < esty + 250u) esty = a > esty ? min(a, esty + 30u) : esty - ((esty - a) >> 3);
          if (b > 500u && b < estr + 250u) estr = b > estr ? min(b, estr + 30u) : estr - ((estr - b) >> 3);
          const float tpc = (float)(rclk() - t00) * __builtin_amdgcn_rcpf((float)(cclk() - c00));
          c1 = __builtin_amdgcn_readfirstlane((unsigned)((float)esty * tpc)) + PACE_C1;
          c3 = min(__builtin_amdgcn_readfirstlane((unsigned)((float)estr * tpc)) + PACE_C3, 250u);
        }
      }
    }
    else s1 = idx - 125;
  } else {
    if (idx < 72) {
      role_wqk(p, ws, rw, idx, lane_id(), w);
      if (idx < 54) {
        const int last = idx >= 27, q = idx - 27 * last, a0 = last ? NSEG - 1 : S3_SPLIT, a1 = last ? NSEG : NSEG - 1;
        const unsigned f3 = C_HFLAG + 20 * last, f2 = f3 + 12;
        if (q < 12) role_mix<3, 1, 5, false>(p, ws, rw, q, a0, a1, f3, f2, lane_id(), w);
        else if (q < 17) role_mix<2, 1, 5, false>(p, ws, rw, q - 12, a0, a1, f2, C_S1FLAG, lane_id(), w);
        else { s4 = q - 17; s40 = a0; s41 = a1; s4f = f3; s4n = 12; }
      }
    }
    else if (idx < 104) role_v2(p, ws, rw, idx - 72, lane_id(), w);
    else if (idx < 114) s4 = idx - 104;
    else if (idx < 120) role_mix<3, 2, 5, true>(p, ws, rw, idx - 114, 0, S3_SPLIT, C_S3FLAG, C_S2FLAG, lane_id(), w);
    else if (idx < 125) role_mix<2, 1, 5, true>(p, ws, rw, idx - 120, 0, S3_SPLIT, C_S2FLAG, C_S1FLAG, lane_id(), w);
    else if (idx < 127) s1 = idx - 122;
  }
  if (s1 >= 0) role_mix<1, 1, 20, true>(p, ws, rw, s1, 0, NSEG, C_S1FLAG, C_TFLAG, lane_id(), w);
  if (s4 >= 0) role_mix4(p, ws, rw, s4, s40, s41, s4f, s4n, lane_id(), w);
}

__global__ void k_end() {}

extern "C" void kernel_launch(void* const* d_in, const int* in_sizes, int n_in, void* d_out, int out_size, void* d_ws, size_t ws_size,
                              hipStream_t stream) {
  Params p;
  p.x = (const unsigned short*)d_in[0]; p.mem0 = (const unsigned short*)d_in[1];
  p.wq = (const unsigned short*)d_in[2]; p.wk = (const unsigned short*)d_in[3]; p.wv = (const unsigned short*)d_in[4]; p.wo = (const unsigned short*)d_in[5];
  p.w1 = (const unsigned short*)d_in[8]; p.w2 = (const unsigned short*)d_in[9]; p.w3 = (const unsigned short*)d_in[10]; p.w4 = (const unsigned short*)d_in[11];
  p.out = (unsigned short*)d_out; p.ws = (unsigned char*)d_ws;
  (void)hipFuncSetAttribute((const void*)k_main, hipFuncAttributeMaxDynamicSharedMemorySize, LDS_BYTES);
  k_init<<<INIT_CAL + INIT_CODE + INIT_FILL, 256, 0, stream>>>(p);
  k_main<<<256, 256, LDS_BYTES, stream>>>(p);
}
```

```cpp
#include <hip/hip_runtime.h>
#include <hip/hip_bf16.h>
#include <type_traits>
#include <utility>

constexpr int S_ = 2048, D_ = 640, H_ = 32, E_ = 192, HE_ = H_ * E_, NSEG = 64, F_ = 1536;

#define LAS __attribute__((address_space(3)))
typedef unsigned v4u __attribute__((ext_vector_type(4)));
typedef unsigned v2u __attribute__((ext_vector_type(2)));
typedef unsigned v8u __attribute__((ext_vector_type(8)));
typedef float f4 __attribute__((ext_vector_type(4)));
typedef float f2 __attribute__((ext_vector_type(2)));
typedef float f16v __attribute__((ext_vector_type(16)));
typedef __bf16 bf2v __attribute__((ext_vector_type(2)));
typedef __bf16 bf8v __attribute__((ext_vector_type(8)));
typedef __bf16 bf16v __attribute__((ext_vector_type(16)));
typedef short s4v __attribute__((ext_vector_type(4)));
typedef __amdgpu_buffer_rsrc_t rsrc_t;

constexpr unsigned MB = 1u << 20;
constexpr unsigned WS_HO = 0;
constexpr unsigned WS_UM = 4 * MB;
constexpr unsigned WS_CTRL = 6 * MB;
constexpr unsigned WS_CAL = 6 * MB + 128 * 1024;
constexpr unsigned WS_REP = 6 * MB + 256 * 1024;
constexpr unsigned WS_PX = 8 * MB;
constexpr unsigned WS_MC = 12 * MB;
constexpr unsigned WS_XF = 18 * MB;
constexpr unsigned WS_HF = 21 * MB;
constexpr unsigned WS_H1F = 24 * MB;
constexpr unsigned WS_X1F = 27 * MB;
constexpr unsigned WS_H2F = 30 * MB;
constexpr unsigned WS_ST = 37 * MB;
constexpr unsigned WS_XST = 37 * MB + 65536;
constexpr unsigned WS_QKV = 38 * MB;
constexpr unsigned WS_UX = 86 * MB;
constexpr unsigned WS_UMT = 166 * MB;
#define SPL 44
constexpr int S3_SPLIT = SPL;
#define WEAG 4
#define VEAG 1
#define LEAD0 16
#define LEADS 32
#define RNG 16
constexpr int RING = RNG, W_EAGER = WEAG, V_EAGER = VEAG, NSETUP = 88, ARM = 3;
constexpr unsigned WS_END = 212 * MB;

constexpr unsigned WS_DBG = 211 * MB;
constexpr unsigned HO_P = 0, HO_MEM = 20480, HO_E = 30720;
#define PACE_C1 14
#define PACE_C3 8
#define PACE_UM 25
#define PACE_LATE 28
constexpr unsigned C_TICKET = 0, C_TMO = 9, C_SET1 = 10, C_WFLAG = 16, C_UFLAG = 96, C_TFLAG = 168, C_S1FLAG = 128, C_S2FLAG = 200, C_S3FLAG = 208, C_HFLAG = 216;
constexpr unsigned SENT = 0xFFFFFFFFu;
constexpr unsigned SPINCAP = 1u << 18;
constexpr int LDS_BYTES = 160 * 1024;

struct Params {
  const unsigned short* x; const unsigned short* mem0;
  const unsigned short* wq; const unsigned short* wk; const unsigned short* wv; const unsigned short* wo;
  const unsigned short* w1; const unsigned short* w2; const unsigned short* w3; const unsigned short* w4;
  unsigned short* out; unsigned char* ws;
};

__device__ __forceinline__ unsigned char* ws_base(unsigned char* ws) {
  return ws + ((0u - (unsigned)(size_t)ws) & (2 * MB - 1));
}
__device__ __forceinline__ rsrc_t mk_rsrc(const void* p, unsigned bytes) {
  return __builtin_amdgcn_make_buffer_rsrc((void*)p, 0, bytes, 0x00020000);
}
__device__ __forceinline__ v4u ld16c(rsrc_t r, unsigned v, unsigned s) { return __builtin_amdgcn_raw_buffer_load_b128(r, v, s, 16); }
__device__ __forceinline__ unsigned ld4c(rsrc_t r, unsigned v, unsigned s) { return __builtin_amdgcn_raw_buffer_load_b32(r, v, s, 16); }
__device__ __forceinline__ void st16c(rsrc_t r, unsigned v, unsigned s, v4u d) { __builtin_amdgcn_raw_buffer_store_b128(d, r, v, s, 16); }
__device__ __forceinline__ void st8c(rsrc_t r, unsigned v, unsigned s, v2u d) { __builtin_amdgcn_raw_buffer_store_b64(d, r, v, s, 16); }
__device__ __forceinline__ void st4c(rsrc_t r, unsigned v, unsigned s, unsigned d) { __builtin_amdgcn_raw_buffer_store_b32(d, r, v, s, 16); }
__device__ __forceinline__ void st2c(rsrc_t r, unsigned v, unsigned s, unsigned short d) { __builtin_amdgcn_raw_buffer_store_b16(d, r, v, s, 16); }
__device__ __forceinline__ v4u ld16p(rsrc_t r, unsigned v, unsigned s) { return __builtin_amdgcn_raw_buffer_load_b128(r, v, s, 0); }
__device__ __forceinline__ unsigned ld4p(rsrc_t r, unsigned v, unsigned s) { return __builtin_amdgcn_raw_buffer_load_b32(r, v, s, 0); }

__device__ __forceinline__ unsigned f2u(float a) { return __builtin_bit_cast(unsigned, a); }
__device__ __forceinline__ float u2f(unsigned a) { return __builtin_bit_cast(float, a); }
__device__ __forceinline__ unsigned pk2(float a, float b) { bf2v t = {(__bf16)a, (__bf16)b}; return __builtin_bit_cast(unsigned, t); }
__device__ __forceinline__ unsigned pk2a(float a, float b) { unsigned r; asm("v_cvt_pk_bf16_f32 %0, |%1|, |%2|" : "=v"(r) : "v"(a), "v"(b)); return r; }
__device__ __forceinline__ float bflo(unsigned u) { return __builtin_bit_cast(float, u << 16); }
__device__ __forceinline__ float bfhi(unsigned u) { return __builtin_bit_cast(float, u & 0xFFFF0000u); }
__device__ __forceinline__ float rbf(float a) { return (float)(__bf16)a; }
__device__ __forceinline__ v4u pk8(f4 a, f4 b) { return (v4u){pk2(a[0], a[1]), pk2(a[2], a[3]), pk2(b[0], b[1]), pk2(b[2], b[3])}; }

__device__ __forceinline__ f4 mfma32(v4u a, v4u b, f4 c) {
  return __builtin_amdgcn_mfma_f32_16x16x32_bf16(__builtin_bit_cast(bf8v, a), __builtin_bit_cast(bf8v, b), c, 0, 0, 0);
}
__device__ __forceinline__ f4 smfmac64(v4u a, v8u b, f4 c, int idx) {
  return __builtin_amdgcn_smfmac_f32_16x16x64_bf16(__builtin_bit_cast(bf8v, a), __builtin_bit_cast(bf16v, b), c, idx, 0, 0);
}
template <bool FIRST, bool WA, bool WB> __device__ __forceinline__ void mfma_big(f16v& d, v4u a, v4u b) {
  if (FIRST) {
    if (WA) asm volatile("v_mfma_f32_32x32x16_bf16 %0, %1, %2, 0" : "=&v"(d) : "a"(a), "v"(b));
    else if (WB) asm volatile("v_mfma_f32_32x32x16_bf16 %0, %1, %2, 0" : "=&v"(d) : "v"(a), "a"(b));
    else asm volatile("v_mfma_f32_32x32x16_bf16 %0, %1, %2, 0" : "=&v"(d) : "v"(a), "v"(b));
  } else {
    if (WA) asm volatile("v_mfma_f32_32x32x16_bf16 %0, %1, %2, %0" : "+v"(d) : "a"(a), "v"(b));
    else if (WB) asm volatile("v_mfma_f32_32x32x16_bf16 %0, %1, %2, %0" : "+v"(d) : "v"(a), "a"(b));
    else asm volatile("v_mfma_f32_32x32x16_bf16 %0, %1, %2, %0" : "+v"(d) : "v"(a), "v"(b));
  }
}
#define PAD_BIG() do { __builtin_amdgcn_sched_barrier(0); asm volatile("s_nop 7\n\ts_nop 3"); __builtin_amdgcn_sched_barrier(0); } while (0)
__device__ __forceinline__ void mfma_v0(f4& d, v4u a, v4u b) { asm volatile("s_nop 1\n\tv_mfma_f32_16x16x32_bf16 %0, %1, %2, 0" : "=&v"(d) : "v"(a), "v"(b)); }
__device__ __forceinline__ void mfma_v(f4& d, v4u a, v4u b) { asm volatile("s_nop 1\n\tv_mfma_f32_16x16x32_bf16 %0, %1, %2, %0" : "+v"(d) : "v"(a), "v"(b)); }
__device__ __forceinline__ float vmax2(float a, float b) { float r; asm("v_max_f32 %0, %1, %2" : "=v"(r) : "v"(a), "v"(b)); return r; }
__device__ __forceinline__ float vmax3(float a, float b, float c) { float r; asm("v_max3_f32 %0, %1, %2, %3" : "=v"(r) : "v"(a), "v"(b), "v"(c)); return r; }
__device__ __forceinline__ v2u trread(unsigned lds_addr) {
  return __builtin_bit_cast(v2u, __builtin_amdgcn_ds_read_tr16_b64_v4i16((LAS s4v*)lds_addr));
}
__device__ __forceinline__ v4u lds16(unsigned a) { return *(LAS v4u*)a; }
__device__ __forceinline__ void lds16w(unsigned a, v4u v) { *(LAS v4u*)a = v; }
__device__ __forceinline__ void lds4w(unsigned a, unsigned v) { *(LAS unsigned*)a = v; }
__device__ __forceinline__ unsigned lds4(unsigned a) { return *(LAS unsigned*)a; }

__device__ __forceinline__ void glds16(const void* gsrc, unsigned dst) {
  unsigned keep;
  asm volatile("s_mov_b32 %0, m0\n\ts_mov_b32 m0, %2\n\ts_nop 0\n\tglobal_load_lds_dwordx4 %1, off\n\ts_mov_b32 m0, %0"
               : "=&s"(keep) : "v"(gsrc), "s"(dst) : "memory");
}
#define VMWAIT(n) asm volatile("s_waitcnt vmcnt(" #n ")" ::: "memory")
__device__ __forceinline__ void bar() {
  asm volatile("s_waitcnt lgkmcnt(0)" ::: "memory");
  __builtin_amdgcn_s_barrier();
  asm volatile("" ::: "memory");
}
__device__ __forceinline__ unsigned nosent(unsigned v) { return v < 0xFFFFFFFEu ? v : 0xFFFFFFFEu; }
__device__ __forceinline__ unsigned mx4(v4u v) { return max(max(v.x, v.y), max(v.z, v.w)); }
__device__ __forceinline__ unsigned or4(v4u v) { return (v.x | v.y) | (v.z | v.w); }
__device__ __forceinline__ unsigned ho_phys(unsigned o) { return ((o >> 13) << 14) | (o & 8191u); }

__device__ __forceinline__ float xor16_max(float v) { auto r = __builtin_amdgcn_permlane16_swap(f2u(v), f2u(v), false, false);
  return vmax2(u2f(r[0]), u2f(r[1])); }
__device__ __forceinline__ float xor32_max(float v) { auto r = __builtin_amdgcn_permlane32_swap(f2u(v), f2u(v), false, false);
  return vmax2(u2f(r[0]), u2f(r[1])); }
__device__ __forceinline__ float xor16_add(float v) { auto r = __builtin_amdgcn_permlane16_swap(f2u(v), f2u(v), false, false);
  return u2f(r[0]) + u2f(r[1]); }
__device__ __forceinline__ float xor32_add(float v) { auto r = __builtin_amdgcn_permlane32_swap(f2u(v), f2u(v), false, false);
  return u2f(r[0]) + u2f(r[1]); }
__device__ __forceinline__ float ror8(float v) {
  return __builtin_bit_cast(float, __builtin_amdgcn_update_dpp(0, __builtin_bit_cast(int, v), 0x128, 0xf, 0xf, true));
}
__device__ __forceinline__ void row16_max(float& a, float& b) {
  asm volatile("s_nop 1\n\t"
               "v_max_f32_dpp %0, %0, %0 row_ror:8 row_mask:0xf bank_mask:0xf\n\tv_max_f32_dpp %1, %1, %1 row_ror:8 row_mask:0xf bank_mask:0xf\n\ts_nop 0\n\t"
               "v_max_f32_dpp %0, %0, %0 row_ror:4 row_mask:0xf bank_mask:0xf\n\tv_max_f32_dpp %1, %1, %1 row_ror:4 row_mask:0xf bank_mask:0xf\n\ts_nop 0\n\t"
               "v_max_f32_dpp %0, %0, %0 row_ror:2 row_mask:0xf bank_mask:0xf\n\tv_max_f32_dpp %1, %1, %1 row_ror:2 row_mask:0xf bank_mask:0xf\n\ts_nop 0\n\t"
               "v_max_f32_dpp %0, %0, %0 row_ror:1 row_mask:0xf bank_mask:0xf\n\tv_max_f32_dpp %1, %1, %1 row_ror:1 row_mask:0xf bank_mask:0xf"
               : "+v"(a), "+v"(b));
}
__device__ __forceinline__ void row16_add(float& a, float& b) {
  asm volatile("s_nop 1\n\t"
               "v_add_f32_dpp %0, %0, %0 row_ror:8 row_mask:0xf bank_mask:0xf\n\tv_add_f32_dpp %1, %1, %1 row_ror:8 row_mask:0xf bank_mask:0xf\n\ts_nop 0\n\t"
               "v_add_f32_dpp %0, %0, %0 row_ror:4 row_mask:0xf bank_mask:0xf\n\tv_add_f32_dpp %1, %1, %1 row_ror:4 row_mask:0xf bank_mask:0xf\n\ts_nop 0\n\t"
               "v_add_f32_dpp %0, %0, %0 row_ror:2 row_mask:0xf bank_mask:0xf\n\tv_add_f32_dpp %1, %1, %1 row_ror:2 row_mask:0xf bank_mask:0xf\n\ts_nop 0\n\t"
               "v_add_f32_dpp %0, %0, %0 row_ror:1 row_mask:0xf bank_mask:0xf\n\tv_add_f32_dpp %1, %1, %1 row_ror:1 row_mask:0xf bank_mask:0xf"
               : "+v"(a), "+v"(b));
}
__device__ __forceinline__ unsigned fold_pack(f4 a) { return pk2(a[0] + a[2], a[1] + a[3]); }
__device__ __forceinline__ int sparse_idx(int lane) { return (lane & 2) ? 0xEEEE : 0x4444; }
__device__ __forceinline__ unsigned cclk() { return (unsigned)__builtin_amdgcn_s_memtime(); }
__device__ __forceinline__ unsigned rclk() { return (unsigned)__builtin_amdgcn_s_memrealtime(); }
__device__ __forceinline__ void wait_until(unsigned t) {
  unsigned d, cnt = 8192;
  asm volatile("1:\n\t"
               "s_memrealtime vcc\n\t"
               "s_sub_u32 %1, %1, 1\n\t"
               "s_cbranch_scc1 3f\n\t"
               "s_waitcnt lgkmcnt(0)\n\t"
               "s_sub_u32 %0, vcc_lo, %2\n\t"
               "s_bitcmp1_b32 %0, 23\n\t"
               "s_cbranch_scc1 1b\n\t"
               "s_and_b32 %0, %0, 0xffffff\n\t"
               "s_cmp_ge_u32 %0, 3\n\ts_cbranch_scc1 3f\n\ts_nop 3\n\t"
               "s_cmp_ge_u32 %0, 2\n\ts_cbranch_scc1 3f\n\ts_nop 3\n\t"
               "s_cmp_ge_u32 %0, 1\n\ts_cbranch_scc1 3f\n\ts_nop 10\n\t"
               "3:\n\t"
               "s_waitcnt lgkmcnt(0)"
               : "=&s"(d), "+s"(cnt) : "s"(__builtin_amdgcn_readfirstlane(t - 4u)) : "scc", "vcc", "memory");
}
__device__ __forceinline__ int lane_id() { int l; asm volatile("v_mbcnt_lo_u32_b32 %0, -1, 0\n\tv_mbcnt_hi_u32_b32 %0, -1, %0" : "=v"(l)); return l & 63; }
__device__ __forceinline__ unsigned sopaque(unsigned v) { asm volatile("" : "+s"(v)); return v; }
#define STAMP(id, slot) do {} while (0)
#define STAMPV(id, slot, v) do {} while (0)
#define MARK(id, s) do {} while (0)
__device__ __forceinline__ unsigned opaque(unsigned v) { asm volatile("" : "+v"(v)); return v; }
template <class F, int... I> __device__ __forceinline__ void static_for_impl(F&& f, std::integer_sequence<int, I...>) { (f(std::integral_constant<int, I>{}), ...); }
template <int N, class F> __device__ __forceinline__ void static_for(F&& f) { static_for_impl(f, std::make_integer_sequence<int, N>{}); }
#define CYC(x) do {} while (0)
#define SB() __builtin_amdgcn_sched_barrier(0)
#define PIN_A(x) asm volatile("" : "+a"(x))
__device__ __forceinline__ unsigned wait_word(rsrc_t rw, unsigned off, bool& dead) {
  unsigned v = 0;
  for (unsigned sp = 0; !dead; ++sp) {
    asm volatile("" ::: "memory");
    v = __builtin_amdgcn_readfirstlane(ld4c(rw, 0, off));
    if (v != SENT) break;
    if (sp > SPINCAP) { dead = true; st4c(rw, 0, WS_CTRL + C_TMO * 256, 5000); }
  }
  return v;
}
__device__ __forceinline__ unsigned stripe_word(rsrc_t rw, int lane) {
  int v = (int)ld4p(rw, (lane & 15) * 4, WS_CAL);
  v += __shfl_xor(v, 1); v += __shfl_xor(v, 2); v += __shfl_xor(v, 4); v += __shfl_xor(v, 8);
  return __builtin_amdgcn_readfirstlane(v) > 0 ? 13u : 2u;
}
__device__ __forceinline__ unsigned stripe(unsigned base, unsigned o, unsigned p0) {
  const unsigned t = base + ((o >> 13) << 14) + (o & 8191u);
  return t | (((p0 ^ (t >> 21)) & 1u) << 13);
}
#define HOME_QK 0
#define HOME_UX 0
__device__ __forceinline__ unsigned qk_at(int s, int h, int frag, unsigned p0) { const unsigned o = (unsigned)(((s & (RING - 1)) * 32 + h) * 24 + frag) * 1024u;
  return stripe(WS_QKV, o, HOME_QK == 2 ? p0 ^ (o >> 13) : p0 ^ HOME_QK); }
__device__ __forceinline__ unsigned ux_at(int s, int c, int h, unsigned p0) { const unsigned o = (unsigned)(((s & (RING - 1)) * 40 + c) * 32 + h) * 1024u;
  return stripe(WS_UX, o, HOME_UX == 2 ? p0 ^ (o >> 13) : p0 ^ HOME_UX); }
__device__ __forceinline__ unsigned px_at(int s, int frag, unsigned p0) { return stripe(WS_PX, (unsigned)((s & (RING - 1)) * 80 + frag) * 1024u, p0); }
__device__ __forceinline__ unsigned umt_at(int s, int c, unsigned p0) { return stripe(WS_UMT, (unsigned)((s & (RING - 1)) * 40 + c) * 8192u, p0); }
__device__ __forceinline__ void report(rsrc_t rw, int id, int s, unsigned cyc, unsigned p0) { st4c(rw, 0, stripe(WS_REP, id * 128u, p0), ((unsigned)s << 16) | min(cyc, 65535u)); }
__device__ __forceinline__ unsigned mem_at(int s, int q, unsigned o, unsigned p0) { return q ? stripe(WS_MC, (unsigned)s * 32768u + (q - 1) * 10240u + o, p0) : stripe(WS_HO, (unsigned)s * 32768u + HO_MEM + o, p0); }
__device__ __forceinline__ unsigned ho_base(int s, unsigned pol) { return WS_HO + (unsigned)s * 65536u + (((pol >> (s >> 5)) & 1u) << 13); }
__device__ __forceinline__ int wait_ge(rsrc_t rw, unsigned word, int want, bool& dead) {
  int v = -1;
  for (unsigned sp = 0; !dead; ++sp) {
    asm volatile("" ::: "memory");
    v = (int)__builtin_amdgcn_readfirstlane(ld4c(rw, 0, WS_CTRL + word * 256));
    if (v >= want) break;
    if (sp > SPINCAP) { dead = true; st4c(rw, 0, WS_CTRL + C_TMO * 256, word); }
    __builtin_amdgcn_s_sleep(8);
  }
  __builtin_amdgcn_fence(__ATOMIC_ACQUIRE, "wavefront");
  return v;
}

struct Hint {
  unsigned off; int v, cls;
  __device__ __forceinline__ Hint(unsigned word, int cnt, int lane) : off((word + (unsigned)(lane < cnt ? lane : 0)) * 256u), v(-1), cls(cnt == 15 ? (lane < 15 ? lane / 5 : 0) : -1) {}
  __device__ __forceinline__ void sample(rsrc_t rw) { asm volatile("" ::: "memory"); v = (int)ld4c(rw, off, WS_CTRL); }
  __device__ __forceinline__ bool has(int s) const {
    const int l = s - ((s ^ cls ^ 1) & 1);
    return __all(v >= (cls < 0 ? s : cls == 0 ? min(s, S3_SPLIT - 1) : l >= S3_SPLIT ? l : -1));
  }
  __device__ __forceinline__ void need(rsrc_t rw, int s, bool& dead) {
    for (unsigned sp = 0; !has(s) && !dead; ++sp) {
      if (sp) __builtin_amdgcn_s_sleep(2);
      sample(rw);
      if (sp > SPINCAP) { dead = true; st4c(rw, 0, WS_CTRL + C_TMO * 256, 7000 + s); }
    }
    __builtin_amdgcn_fence(__ATOMIC_ACQUIRE, "wavefront");
  }
};

constexpr int INIT_CAL = 16, INIT_CODE = 16, INIT_FILL = 5;
__global__ void __launch_bounds__(256) k_init(Params p) {
  unsigned char* ws = ws_base(p.ws);
  const int tid = threadIdx.x, lane = tid & 63, w = tid >> 6;
  int b = blockIdx.x;
  if (b < INIT_CAL) {
    if (w) return;
    const unsigned char* a = ws + (5 + 2 * (b & 1)) * MB + 512 * 1024 + b * 16384 + (lane & 31) * 4;
    unsigned best[2] = {~0u, ~0u};
    for (int k = 0; k < 2; ++k)
#pragma unroll
      for (int st = 0; st < 2; ++st) {
        const unsigned char* q = a + st * 8192;
        unsigned long long t0, t1; unsigned val = 0;
        asm volatile("s_memtime %0\n\ts_waitcnt lgkmcnt(0)\n\tglobal_atomic_add %2, %3, %2, off sc0\n\ts_waitcnt vmcnt(0)\n\ts_memtime %1\n\ts_waitcnt lgkmcnt(0)"
                     : "=&s"(t0), "=&s"(t1), "+v"(val) : "v"(q) : "memory");
        if (k) best[st] = min(best[st], (unsigned)(t1 - t0));
      }
    const unsigned flip = (b ^ ((unsigned)__builtin_amdgcn_s_getreg(20 | (3 << 11)) >> 2)) & 1u;
    const int dlt = (int)best[0] - (int)best[1];
    if (lane == 0) *(int*)(ws + WS_CAL + b * 4) = flip ? -dlt : dlt;
    return;
  }
  b -= INIT_CAL;
  if (b < INIT_CODE) {
    unsigned long long a0, a1;
    asm volatile("s_getpc_b64 vcc\n\ts_add_u32 vcc_lo, vcc_lo, _Z6k_main6Params@rel32@lo+4\n\ts_addc_u32 vcc_hi, vcc_hi, _Z6k_main6Params@rel32@hi+12\n\ts_mov_b64 %0, vcc"
                 : "=s"(a0) :: "vcc", "scc");
    asm volatile("s_getpc_b64 vcc\n\ts_add_u32 vcc_lo, vcc_lo, _Z5k_endv@rel32@lo+4\n\ts_addc_u32 vcc_hi, vcc_hi, _Z5k_endv@rel32@hi+12\n\ts_mov_b64 %0, vcc"
                 : "=s"(a1) :: "vcc", "scc");
    const unsigned long long lo = a0 < a1 ? a0 : a1;
    rsrc_t rc = mk_rsrc((const void*)lo, (unsigned)((a0 < a1 ? a1 : a0) - lo));
    const unsigned v = ld4p(rc, ((unsigned)b * 256u + tid) * 128u, 0);
    asm volatile("" :: "v"(v));
    return;
  }
  b -= INIT_CODE;
#pragma unroll
  for (int k = 0; k < 4; ++k) *(v4u*)(ws + WS_CTRL + b * 16384 + k * 4096 + tid * 16) = (v4u){SENT, SENT, SENT, SENT};
}

__device__ __forceinline__ void setup_job(const Params& p, unsigned char* ws, rsrc_t rw, int k0, int lane, int w) {
  const int g = lane >> 4, n = lane & 15;
  const unsigned p0 = stripe_word(rw, lane) >> 3, l16 = lane * 16u;
  const v4u sent = {SENT, SENT, SENT, SENT};
  if (k0 < NSEG) {
    v4u v[2][5]; float mu[2], t[2];
#pragma unroll
    for (int i = 0; i < 10; ++i) v[i & 1][i >> 1] = *(const v4u*)(p.x + (size_t)(k0 * 32 + 16 * (n >> 3) + 8 * (i & 1) + (n & 7)) * D_ + (w * 5 + (i >> 1)) * 32 + g * 8);
    auto total = [&](int slot) {
      if (g == 0) *(LAS f2*)(slot * 512 + w * 128 + n * 8) = (f2){t[0], t[1]};
      bar();
      const unsigned a = slot * 512 + n * 8;
      const f2 r = (*(LAS f2*)a + *(LAS f2*)(a + 128)) + (*(LAS f2*)(a + 256) + *(LAS f2*)(a + 384));
      t[0] = r[0] * (1.f / D_); t[1] = r[1] * (1.f / D_);
    };
#pragma unroll
    for (int rt = 0; rt < 2; ++rt) {
      float sum = 0.f;
#pragma unroll
      for (int e = 0; e < 20; ++e) sum += bflo(v[rt][e >> 2][e & 3]) + bfhi(v[rt][e >> 2][e & 3]);
      t[rt] = xor32_add(xor16_add(sum));
    }
    total(0);
#pragma unroll
    for (int rt = 0; rt < 2; ++rt) {
      mu[rt] = t[rt];
      float sq = 0.f;
#pragma unroll
      for (int e = 0; e < 20; ++e) { const float a = bflo(v[rt][e >> 2][e & 3]) - mu[rt], c = bfhi(v[rt][e >> 2][e & 3]) - mu[rt]; sq += a * a + c * c; }
      t[rt] = xor32_add(xor16_add(sq));
    }
    total(1);
#pragma unroll
    for (int rt = 0; rt < 2; ++rt) {
      const float mub = rbf(mu[rt]), sd = rbf(sqrtf(rbf(rbf(t[rt]) + 1e-5f)));
#pragma unroll
      for (int k = 0; k < 5; ++k) {
        v4u o;
#pragma unroll
        for (int j = 0; j < 4; ++j) o[j] = pk2(rbf(bflo(v[rt][k][j]) - mub) / sd, rbf(bfhi(v[rt][k][j]) - mub) / sd);
        st16c(rw, l16, WS_XF + (unsigned)((k0 * 20 + w * 5 + k) * 2 + rt) * 1024u, o);
        st16c(rw, l16, WS_HF + (unsigned)((k0 * 20 + w * 5 + k) * 2 + rt) * 1024u, sent);
      }
    }
  } else {
    for (unsigned i = (k0 - NSEG) * 4 + w; i < 2688u + 80u + 2048u; i += (NSETUP - NSEG) * 4) {
      v4u v = sent;
      const unsigned mi = i < 2688u ? i - HO_MEM / 1024 : (i - 2768u < 30u ? (i - 2768u) % 10u : 99u);
      if (mi < 10u) {
        const unsigned* m32 = (const unsigned*)p.mem0 + (2 * (n >> 2) + (n & 1)) * 320 + 8 * (mi * 4 + g) + ((n >> 1) & 1);
#pragma unroll
        for (int q = 0; q < 4; ++q) v[q] = nosent(m32[2 * q]);
      }
      st16c(rw, l16, i < 2688u ? stripe(WS_HO, i * 1024u, p0) : i < 2768u ? stripe(WS_PX, (i - 2688u) * 1024u, p0) : stripe(WS_MC, (i - 2768u) * 1024u, p0), v);
    }
  }
  VMWAIT(0);
  bar();
  if (w == 0 && lane == 0) __hip_atomic_fetch_add((unsigned*)(ws + WS_CTRL + C_SET1 * 256), 1u, __ATOMIC_RELAXED, __HIP_MEMORY_SCOPE_AGENT);
  if (w == 1 && k0 < 60) STAMP(64 + k0, 2);
}

__device__ __forceinline__ void role_wqk(const Params& p, unsigned char* ws, rsrc_t rw, int j, int lane, int w) {
  const int wu = 4 * j + w;
  const int h = wu < 192 ? wu / 6 : (wu - 192) / 3, qk = wu < 192 ? (wu % 6) / 3 : 2, e3 = wu % 3;
  const int gg = lane >> 4, hi = lane >> 5, r = (lane >> 2) & 3, cc = lane & 3;
  const int dr = lane >> 3, c16 = lane & 7, fs = ((dr >> 1) & 1);
  const unsigned short* W = (qk == 0 ? p.wq : qk == 1 ? p.wk : p.wv) + h * E_ + e3 * 64;
  const unsigned lb = w * 40960;
  auto issue = [&](int blk) {
#pragma unroll
    for (int i = 0; i < 8; ++i) {
      const int f = fs | ((i & 1) << 1);
      glds16(W + (size_t)(blk * 64 + i * 8 + dr) * HE_ + (((c16 >> 1) ^ f) * 16) + (c16 & 1) * 8, lb + (blk % 5) * 8192 + i * 1024);
    }
  };
#pragma unroll
  for (int b = 0; b < 5; ++b) issue(b);
  v4u wt[2][40]; unsigned p0 = 0; bool dead = false;
  const int f = (r >> 1) | (hi << 1);
  const unsigned tb0 = opaque(lb + (8 * hi + r) * 128 + (((gg & 1) ^ f) * 32) + cc * 8), tb1 = opaque(lb + (8 * hi + r) * 128 + (((2 + (gg & 1)) ^ f) * 32) + cc * 8);
#pragma unroll
  for (int b = 0; b < 10; ++b) {
    if (b <= 5) VMWAIT(32); else if (b == 6) VMWAIT(24);
    else if (b == 7) {
      p0 = (stripe_word(rw, lane) >> 3) ^ (qk == 2 ? 1u : 0u);
      VMWAIT(0);
      if (w == 0) { wait_ge(rw, C_SET1, NSETUP - 1, dead); __builtin_amdgcn_fence(__ATOMIC_ACQUIRE, "agent"); }
    }
#pragma unroll
    for (int t = 0; t < 2; ++t)
#pragma unroll
      for (int k = 0; k < 4; ++k) { const unsigned a = (t ? tb1 : tb0) + (b % 5) * 8192 + k * 2048; v2u lo = trread(a), hi2 = trread(a + 512); wt[t][4 * b + k] = (v4u){lo.x, lo.y, hi2.x, hi2.y}; }
    asm volatile("s_waitcnt lgkmcnt(0)" ::: "memory");
    if (b + 5 < 10) issue(b + 5);
  }
#pragma unroll
  for (int k = 0; k < 32; ++k) { PIN_A(wt[0][k]); PIN_A(wt[1][k]); }
  if (w == 1) STAMP(128 + j, 2);
  VMWAIT(0);
  bar();
  const int ofr = (qk == 1 ? 12 : 0) + e3 * 4;
  Hint done(C_TFLAG, 20, lane);
  const int flagword = C_WFLAG + j;
  const unsigned char* xsrc = ws + WS_XF + w * 10240 + lane * 16;
#pragma unroll
  for (int i = 0; i < 10; ++i) glds16(xsrc + i * 1024, w * 10240 + i * 1024);
#pragma unroll
  for (int i = 0; i < 10; ++i) glds16(xsrc + 40960 + i * 1024, 40960 + w * 10240 + i * 1024);
  VMWAIT(10);
  bar();
  const unsigned xo = (gg & 1) * 1024 + hi * 256 + (lane & 15) * 16;
  auto xoff = [](int k) { return (k >> 1) * 2048 + (k & 1) * 512; };
  v4u xr[4];
  xr[0] = lds16(xo); xr[1] = lds16(xo + xoff(1)); xr[2] = lds16(xo + xoff(2));
  f16v accA[2], accB[2];
#pragma unroll
  for (int i = 0; i < 16; ++i) { accB[0][i] = 0.f; accB[1][i] = 0.f; }
  auto put = [&](const f16v (&acc)[2], int q, int s) {
    const f16v a = acc[q >> 1]; const int o = 4 * (q & 1);
    st16c(rw, xo, qk_at(s, h, ofr, p0) + (q >> 1) * 2048 + (q & 1) * 512,
          (v4u){pk2(a[o], a[o + 1]), pk2(a[o + 2], a[o + 3]), pk2(a[o + 8], a[o + 9]), pk2(a[o + 10], a[o + 11])});
  };
  int b0 = 0;
  auto seg = [&](f16v (&acc)[2], const f16v (&prev)[2], int s) {
    const int b1 = b0 == 2 ? 0 : b0 + 1, b2 = b1 == 2 ? 0 : b1 + 1;
    const unsigned cur = opaque(xo + b0 * 40960), nxt = opaque(xo + b1 * 40960), dst2 = b2 * 40960 + w * 10240;
    b0 = b1;
    static_for<40>([&](auto KC) {
      constexpr int k = decltype(KC)::value;
      if constexpr (k == 1) {
        if (w == 0) done.need(rw, s - 1 - min(RING, LEAD0 + max(0, s - LEADS) / 2), dead);
        VMWAIT(0);
        bar();
        if (w == 0 && lane == 0 && s >= 2 + W_EAGER) st4c(rw, 0, WS_CTRL + flagword * 256, (unsigned)(s - 2));
        if (w == 0) { STAMP(flagword - C_WFLAG + 128, 16 + s); STAMPV(flagword - C_WFLAG + 128, 128 + s, (unsigned)__builtin_amdgcn_s_memtime()); }
      }
      mfma_big<k == 0, (k < 32), false>(acc[0], wt[0][k], xr[k & 3]);
      xr[(k + 3) & 3] = lds16(k < 37 ? cur + xoff(k + 3) : nxt + xoff(k - 37));
      SB();
      mfma_big<k == 0, (k < 32), false>(acc[1], wt[1][k], xr[k & 3]);
      SB();
      if constexpr (k >= 1 && k < 9 && (k & 1)) { if (s >= 1 + W_EAGER) put(prev, k >> 1, s - 1); }
      if constexpr (k >= 8 && k < 28 && !(k & 1)) glds16(xsrc + (size_t)(s + 2) * 40960 + ((k - 8) >> 1) * 1024, dst2 + ((k - 8) >> 1) * 1024);
      SB();
    });
    if (s < W_EAGER || s == NSEG - 1) {
      if (s >= RING) done.need(rw, s - RING, dead);
      asm volatile("s_nop 7\n\ts_nop 3" : "+v"(acc[0]), "+v"(acc[1]));
#pragma unroll
      for (int q = 0; q < 4; ++q) put(acc, q, s);
      VMWAIT(0);
      bar();
      if (w == 0 && lane == 0) st4c(rw, 0, WS_CTRL + flagword * 256, (unsigned)s);
    }
  };
  for (int s = 0; s < NSEG; s += 2) { seg(accA, accB, s); seg(accB, accA, s + 1); }
}

__device__ __forceinline__ constexpr int slot_grp(int j) { return j == 0 || (j >= 2 && j < 5) ? 0 : 1; }
__device__ __forceinline__ constexpr int slot_rot(int j) { return j < 2 ? 0 : (j < 5 ? j - 1 : j - 4); }
__device__ __forceinline__ int slot_ks(int j, int w) { return j < 8 ? 4 * slot_grp(j) + ((w + slot_rot(j)) & 3) : j; }
struct MemGather {
  unsigned o0, o1, o2;
  __device__ __forceinline__ MemGather(int s, int q, unsigned p0, int w) : o0(sopaque(mem_at(s, q, w * 1024, p0))), o1(sopaque(mem_at(s, q, 4096 + w * 1024, p0))), o2(sopaque(mem_at(s, q, 8192 + w * 512, p0))) {}
  __device__ __forceinline__ void load(rsrc_t rw, int lane, v4u& a0, v4u& a1, v2u& a2) const {
    a0 = ld16c(rw, lane * 16, o0); SB(); a1 = ld16c(rw, lane * 16, o1); SB(); a2 = __builtin_amdgcn_raw_buffer_load_b64(rw, lane * 8, o2, 16); SB();
  }
  static __device__ __forceinline__ bool bad(v4u a0, v4u a1, v2u a2) { return __any(max(max(mx4(a0), mx4(a1)), max(a2.x, a2.y)) == SENT); }
  __device__ __forceinline__ void late(rsrc_t rw, int lane, v4u& a0, v4u& a1, v2u& a2, bool& dead) const {
    for (unsigned sp = 0; !dead; ++sp) {
      asm volatile("" ::: "memory");
      load(rw, lane, a0, a1, a2);
      if (!bad(a0, a1, a2)) break;
      if (sp > SPINCAP) { dead = true; st4c(rw, 0, WS_CTRL + C_TMO * 256, 1000); }
    }
  }
};
__device__ __forceinline__ void blk_src(int h, int lane, int w, unsigned (&soff)[6], unsigned (&own)[6]) {
#pragma unroll
  for (int i = 0; i < 6; ++i) {
    const int pos = (w * 6 + i) * 64 + lane, d = pos / 24, c16 = pos % 24;
    const int et = (c16 >> 1) ^ (((d >> 1) & 1) | (((d >> 3) & 1) << 1));
    soff[i] = (unsigned)(d * HE_ + h * E_ + et * 16 + (c16 & 1) * 8); own[i] = (unsigned)(et / 3);
  }
}

constexpr unsigned Y_IMG = 122880, Y_KX = Y_IMG + 6144, Y_M = Y_KX + 12288, Y_QX = Y_M + 10240;
__device__ __forceinline__ constexpr int y_blk_qk(int i) { return i < 15 ? (i < 8 ? 0 : 1) : ((i - 15) & 1 ? 0 : 1); }
__device__ __forceinline__ constexpr int y_blk_ks(int i) {
  constexpr int qs[8] = {0, 1, 3, 4, 5, 7, 8, 9};
  constexpr int kk[7] = {1, 2, 3, 5, 6, 7, 9};
  return i < 8 ? qs[i] : (i < 15 ? kk[i - 8] : 2 * (i - 15));
}
__device__ __forceinline__ constexpr int y_tail_t(int j) { constexpr int o[24] = {0, 1, 0, 1, 0, 2, 0, 2, 1, 3, 1, 3, 2, 4, 2, 4, 3, 5, 3, 5, 4, 5, 4, 5}; return o[j]; }
__device__ __forceinline__ constexpr int y_tail_k(int j) { int c = 0; for (int i = 0; i < j; ++i) if (y_tail_t(i) == y_tail_t(j)) ++c; return 6 + c; }
__device__ __forceinline__ constexpr int y_find(int qk, int ks) { for (int i = 0; i < 20; ++i) if (y_blk_qk(i) == qk && y_blk_ks(i) == ks) return i; return -1; }

__device__ __forceinline__ void role_y(const Params& p, unsigned char* ws, rsrc_t rw, int h, int lane, int w) {
  const int g = lane >> 4, n = lane & 15, r = (lane >> 2) & 3, cc = lane & 3;
  bool dead = false;
  unsigned soff[6], own[6];
  blk_src(h, lane, w, soff, own);
  auto issue = [&](int i) {
    const unsigned short* W = y_blk_qk(i) ? p.wk : p.wq;
#pragma unroll
    for (int q = 0; q < 6; ++q) glds16(W + soff[q] + (unsigned)slot_ks(y_blk_ks(i), own[q]) * (64u * HE_), (i % 5) * 24576 + (w * 6 + q) * 1024);
  };
  unsigned tb[3];
  { const int f = (r >> 1) | ((g & 1) << 1);
#pragma unroll
    for (int t = 0; t < 3; ++t) tb[t] = (8 * g + r) * 384 + (((3 * w + t) ^ f) * 32) + cc * 8; }
  auto rdfrag = [&](int slot, int t) -> v8u {
    const unsigned a = tb[t] + slot * 24576;
    v2u q0 = trread(a), q1 = trread(a + 4 * 384), q2 = trread(a + 32 * 384), q3 = trread(a + 36 * 384);
    return (v8u){q0.x, q0.y, q1.x, q1.y, q2.x, q2.y, q3.x, q3.y};
  };
#pragma unroll
  for (int i = 0; i < 5; ++i) issue(i);
  v8u wt[15][3];
#pragma unroll
  for (int i = 0; i < 15; ++i) {
    VMWAIT(24);
    bar();
#pragma unroll
    for (int t = 0; t < 3; ++t) wt[i][t] = rdfrag(i % 5, t);
    bar();
#pragma unroll
    for (int t = 0; t < 3; ++t) if (i * 3 + t < 32) PIN_A(wt[i][t]);
    issue(i + 5);
  }
  VMWAIT(0);
  const unsigned pol = stripe_word(rw, lane);
  const unsigned p0 = pol >> 3;
  if (w == 1) STAMP(h, 5);
  wait_ge(rw, C_SET1, NSETUP - 1, dead);
  const int jf0 = C_WFLAG + (6 * h) / 4;
  int seen = -1;
  auto need_qk = [&](int s) {
    for (unsigned sp = 0; seen < s && !dead; ++sp) {
      asm volatile("" ::: "memory");
      const int v = (int)ld4c(rw, (lane & 1) * 256, WS_CTRL + jf0 * 256);
      seen = min(__builtin_amdgcn_readlane(v, 0), __builtin_amdgcn_readlane(v, 1));
      if (sp > SPINCAP) { dead = true; st4c(rw, 0, WS_CTRL + C_TMO * 256, 1500 + s); }
      if (seen < s) __builtin_amdgcn_s_sleep(2);
    }
    __builtin_amdgcn_fence(__ATOMIC_ACQUIRE, "wavefront");
  };
  const unsigned kxb = opaque(Y_KX + lane * 16);
  const unsigned kxp = opaque(Y_KX + (g * 16 + (n & 8) + 4 * (n & 1) + 2 * ((n >> 2) & 1) + ((n >> 1) & 1)) * 16);
  auto load_kx = [&](int s, v4u (&t)[12]) {
#pragma unroll
    for (int i = 0; i < 12; ++i) t[i] = ld16c(rw, lane * 16, qk_at(s, h, 12 + (i & ~3), p0) + (i & 3) * 1024);
  };
  const unsigned qxb = opaque(Y_QX + ((w - 1) & 1) * 6144 + lane * 16);
  auto load_qx = [&](int s, v4u (&q)[6]) {
#pragma unroll
    for (int u = 0; u < 6; ++u) q[u] = ld16c(rw, (n >> 3) * 1024 + (g * 16 + 8 * (w - 1) + (n & 7)) * 16, qk_at(s, h, 4 * (u >> 1), p0) + (u & 1) * 2048);
  };
  if (w == 3) need_qk(1);
  bar();
  const int sidx = sparse_idx(lane);
  const float cexp = 0.07216878364870322f * 1.4426950408889634f;
  const unsigned imwk = opaque(Y_IMG + 3072 + 3 * w * 256 + n * 16 + g * 4);
  unsigned imwq[3];
#pragma unroll
  for (int t = 0; t < 3; ++t) { const int et = 3 * w + t;
    imwq[t] = opaque(Y_IMG + (et >> 2) * 1024 + ((2 * ((et >> 1) & 1) + (n >> 3)) * 16 + 4 * g + 2 * ((n >> 1) & 1)) * 16 + (2 * ((n >> 2) & 1) + (et & 1)) * 4 + (n & 1) * 2); }
  const unsigned imr = opaque(Y_IMG + 3072 + (4 * g + r) * 16 + (cc & 1) * 8), imq = opaque(Y_IMG + lane * 16);
  const unsigned mw = opaque(Y_M + w * 1024 + lane * 16), mw2 = opaque(Y_M + 8192 + w * 512 + lane * 8);
  unsigned mr[4];
#pragma unroll
  for (int i = 1; i < 4; ++i) mr[i] = opaque(Y_M + ((w + i) & 3) * 1024 + lane * 16);
  mr[0] = opaque(Y_M + 8192 + lane * 16);
  unsigned tb2[3];
#pragma unroll
  for (int t = 0; t < 3; ++t) tb2[t] = opaque(tb[t] + 3 * 24576);
  STAMP(h, 1);
  STAMPV(h, 2, (unsigned)__builtin_amdgcn_s_memtime());
  unsigned tpass = rclk();
  bool first = true; unsigned c0 = 0, tb0 = 0, cy[8] = {0, 0, 0, 0, 0, 0, 0, 0}; (void)tb0; (void)first;
  auto next_pace = [&]() {
    const unsigned e = __builtin_amdgcn_readfirstlane(lds4(Y_M + w * 1024)); tpass = (e >> 8) + (e & 255u); };
  v4u qa[3]; f4 d1, d2; v2u kr[12];
  auto proj = [&](auto FIN, unsigned hb, v4u (&kx)[12], auto&& before, auto&& shadow, auto&& tail) {
    constexpr bool fin = decltype(FIN)::value;
    v4u a[10]; v2u a2; f4 acc[6]; v2u sa[3][4], sb[3][4];
    auto cat = [](const v2u (&q)[4]) -> v8u { return (v8u){q[0].x, q[0].y, q[1].x, q[1].y, q[2].x, q[2].y, q[3].x, q[3].y}; };
    auto rdq = [&](int slot, int t, int q) -> v2u {
      return trread((slot < 3 ? tb[t] + slot * 24576 : tb2[t] + (slot - 3) * 24576) + (32 * (q >> 1) + 4 * (q & 1)) * 384); };
    auto rda = [&](int j) -> v4u { return j < 8 ? lds16(mr[slot_rot(j)] + slot_grp(j) * 4096) : lds16(mr[0] + (j - 8) * 1024); };
    auto zero = [&]() {
#pragma unroll
      for (int t = 0; t < 6; ++t) { acc[t] = (f4){0, 0, 0, 0}; asm volatile("" : "+v"(acc[t])); } };
    auto mm = [&](auto GC) {
      constexpr int G = decltype(GC)::value;
      constexpr int ks = G < 36 ? G / 6 : y_tail_k(G < 36 ? 0 : G - 36), t = G < 36 ? G % 6 : y_tail_t(G < 36 ? 0 : G - 36);
      constexpr int bi = y_find(t / 3, ks);
      if constexpr (bi < 15) acc[t] = smfmac64(a[ks], wt[bi][t % 3], acc[t], sidx);
      else acc[t] = smfmac64(a[ks], t < 3 ? cat(sb[t % 3]) : cat(sa[t % 3]), acc[t], sidx);
      SB();
    };
    before();
    SB();
#pragma unroll
    for (int i = 0; i < 12; ++i) sa[i >> 2][i & 3] = rdq(0, i >> 2, i & 3);
    zero();
    const MemGather mg(hb >> 16, h & 3, p0, w);
    SB();
    wait_until(tpass);
    CYC(cy[0]);
    SB();
    mg.load(rw, lane, a[0], a[1], a2);
    c0 = cclk();
    shadow();
    SB();
#pragma unroll
    for (int i = 0; i < 12; ++i) sb[i >> 2][i & 3] = rdq(1, i >> 2, i & 3);
    SB();
    lds16w(mw, a[0]);
    SB();
    mm(std::integral_constant<int, 0>{});
    lds16w(mw + 4096, a[1]);
    SB();
    mm(std::integral_constant<int, 1>{});
    *(LAS v2u*)mw2 = a2;
    SB();
    mm(std::integral_constant<int, 2>{});
    unsigned mx = max(mx4(a[0]), a2.x); asm volatile("" : "+v"(mx)); SB();
    mm(std::integral_constant<int, 3>{});
    mx = max(max(mx, a2.y), mx4(a[1])); asm volatile("" : "+v"(mx)); SB();
    mm(std::integral_constant<int, 4>{});
    first = !__any(mx == SENT);
    mm(std::integral_constant<int, 5>{});
    if (__builtin_expect(!first, 0)) {
      mg.late(rw, lane, a[0], a[1], a2, dead);
      lds16w(mw, a[0]); lds16w(mw + 4096, a[1]); *(LAS v2u*)mw2 = a2;
      zero();
      static_for<6>(mm);
    }
    CYC(cy[1]);
    VMWAIT(0);
    bar();
    CYC(cy[2]);
    SB();
    f2 fs = {0.f, 0.f}; unsigned f0 = 0;
    static_for<54>([&](auto GC0) {
      constexpr int G = decltype(GC0)::value + 6;
      if constexpr (G < 14) a[G - 4] = rda(G - 4);
      else if constexpr (G < 24) { sa[(G - 14) >> 2][(G - 14) & 3] = rdq(2, (G - 14) >> 2, (G - 14) & 3); if constexpr (G >= 22) sa[2][G - 20] = rdq(2, 2, G - 20); }
      else if constexpr (G < 36) sb[(G - 24) >> 2][(G - 24) & 3] = rdq(3, (G - 24) >> 2, (G - 24) & 3);
      else if constexpr (G < 48) sa[(G - 36) >> 2][(G - 36) & 3] = rdq(4, (G - 36) >> 2, (G - 36) & 3);
      else if constexpr (!fin && !(G & 1)) tail((G - 48) >> 1);
      if constexpr (fin && G >= 47 && G < 55) kx[G - 47] = lds16(kxp + (G - 47) * 1024);
      if constexpr (fin && G == 59) { kx[8] = lds16(kxp + 8192); kx[9] = lds16(kxp + 9216); kx[10] = lds16(kxp + 10240); kx[11] = lds16(kxp + 11264); }
      if constexpr (fin && (G == 55 || G == 56)) { (G == 55 ? d1 : d2) = (f4){0, 0, 0, 0}; asm volatile("" : "+v"(G == 55 ? d1 : d2)); }
      if constexpr (G == 58) {
        bar();
        if constexpr (fin) { qa[0] = lds16(imq); qa[1] = lds16(imq + 1024); qa[2] = lds16(imq + 2048); }
      }
      if constexpr (G >= 45) {
        constexpr int tp = (G - 45) / 4, ph = (G - 45) % 4;
        if constexpr (ph == 0) { fs = (f2){acc[tp][0], acc[tp][1]} + (f2){acc[tp][2], acc[tp][3]}; asm volatile("" : "+v"(fs)); }
        if constexpr (ph == 1) { f0 = pk2(fs[0], fs[1]); asm volatile("" : "+v"(f0)); }
        if constexpr (ph == 2) { if constexpr (tp < 3) *(LAS unsigned short*)imwq[tp < 3 ? tp : 0] = (unsigned short)f0; else lds4w(imwk, f0); }
        if constexpr (ph == 3 && tp < 3) *(LAS unsigned short*)(imwq[tp < 3 ? tp : 0] + 16) = (unsigned short)(f0 >> 16);
      }
      SB();
      mm(std::integral_constant<int, G>{});
    });
    CYC(cy[3]);
    auto sx = [&](int i) {
      const int v = i >> 1; const v4u l = kx[4 * v + (i & 1)], h2 = kx[4 * v + 2 + (i & 1)];
      f4& d = (i & 1) ? d2 : d1;
      d = smfmac64(qa[v], (v8u){l.x, l.y, l.z, l.w, h2.x, h2.y, h2.z, h2.w}, d, sidx);
      asm volatile("" : "+v"(d));
      SB();
    };
    lds4w(imwk + 256, fold_pack(acc[4]));
    lds4w(imwk + 512, fold_pack(acc[5]));
    SB();
    if constexpr (fin) { sx(0); sx(1); }
    bar();
    if constexpr (fin) {
#pragma unroll
      for (int i = 0; i < 12; ++i) kr[i] = trread(imr + i * 256);
      SB();
      sx(2); sx(3); sx(4); sx(5);
    }
  };
  if (w == 0) {
    const unsigned pso = (n >> 3) * 256 + (4 * g + 2 * ((n >> 1) & 1) + ((n >> 2) & 1)) * 16 + (n & 1) * 8, pmo = (4 * g + 2 * ((n >> 1) & 1) + (n >> 3)) * 8 + ((n >> 2) & 1) * 4 + (n & 1) * 2;
    for (int s = 0; s < NSEG; ++s) {
      const unsigned hb = ho_base(s, pol);
      const unsigned ox = sopaque(hb + ho_phys(HO_P + (32 + 4 * h) * 128)), om = sopaque(hb + ho_phys(HO_P + h * 128));
      v4u kx[12];
      proj(std::true_type{}, hb, kx, []() {}, []() {}, [](int) {});
      CYC(cy[4]);
      f4 d0 = {0, 0, 0, 0};
      asm volatile("" : "+v"(d0));
      SB();
#pragma unroll
      for (int v = 0; v < 3; ++v)
        d0 = smfmac64(qa[v], (v8u){kr[4 * v].x, kr[4 * v].y, kr[4 * v + 1].x, kr[4 * v + 1].y, kr[4 * v + 2].x, kr[4 * v + 2].y, kr[4 * v + 3].x, kr[4 * v + 3].y}, d0, sidx);
      SB();
      CYC(cy[5]);
      const f2 x0 = (f2){d1[0], d1[1]} + (f2){d1[2], d1[3]}, x1 = (f2){d2[0], d2[1]} + (f2){d2[2], d2[3]}, xm = (f2){d0[0], d0[1]} + (f2){d0[2], d0[3]};
      float ma = vmax3(x0[0], x1[0], xm[0]), mb = vmax3(x0[1], x1[1], xm[1]);
      row16_max(ma, mb);
      const float na = -ma * cexp, nb = -mb * cexp;
      const float e0a = __builtin_amdgcn_exp2f(__builtin_fmaf(x0[0], cexp, na)), e0b = __builtin_amdgcn_exp2f(__builtin_fmaf(x0[1], cexp, nb));
      const float e1a = __builtin_amdgcn_exp2f(__builtin_fmaf(x1[0], cexp, na)), e1b = __builtin_amdgcn_exp2f(__builtin_fmaf(x1[1], cexp, nb));
      const float ema = __builtin_amdgcn_exp2f(__builtin_fmaf(xm[0], cexp, na)), emb = __builtin_amdgcn_exp2f(__builtin_fmaf(xm[1], cexp, nb));
      float sa_ = __builtin_fmaf(ema, 0.5f, e0a + e1a), sb_ = __builtin_fmaf(emb, 0.5f, e0b + e1b);
      const float ems = (n & 8) ? emb : ema;
      row16_add(sa_, sb_);
      const float ra = __builtin_amdgcn_rcpf(sa_), rb = __builtin_amdgcn_rcpf(sb_);
      {
        const unsigned pa = pk2a(e0a * ra, e1a * ra), pb2 = pk2a(e0b * rb, e1b * rb);
        const unsigned lo = (unsigned)__builtin_amdgcn_update_dpp((int)pa, (int)pb2, 0x114, 0xf, 0xa, false);
        const unsigned hi = (unsigned)__builtin_amdgcn_update_dpp((int)pb2, (int)pa, 0x104, 0xf, 0x5, false);
        st8c(rw, pso, ox, (v2u){lo, hi});
      }
      st2c(rw, pmo, om, (unsigned short)pk2a(ems * ((n & 8) ? rb : ra), 0.f));
      SB();
      if (lane == 0) report(rw, h, s, cclk() - c0, p0);
      CYC(cy[6]);
      STAMP(h, 16 + s * 8 + 4);
      bar();
      next_pace();
    }
  } else if (w == 3) {
    v4u t[12];
    load_kx(0, t);
    for (int s = 0; s < NSEG; ++s) {
      const unsigned hb = ho_base(s, pol);
      const int sn = s + 1 < NSEG ? s + 1 : s;
      const unsigned k0 = sopaque(qk_at(sn, h, 12, p0)), k1 = sopaque(qk_at(sn, h, 16, p0)), k2 = sopaque(qk_at(sn, h, 20, p0));
      proj(std::false_type{}, hb, t, [&]() {
#pragma unroll
        for (int i = 0; i < 12; ++i) lds16w(kxb + i * 1024, t[i]); }, []() {}, [&](int i) { t[i] = ld16c(rw, lane * 16 + (i & 3) * 1024, i < 4 ? k0 : k1); });
      unsigned e = ld4c(rw, 0, hb + ho_phys(HO_E));
      SB();
#pragma unroll
      for (int i = 6; i < 12; ++i) t[i] = ld16c(rw, lane * 16 + (i & 3) * 1024, i < 8 ? k1 : k2);
      SB();
      e = __builtin_amdgcn_readfirstlane(e);
      if (e == SENT && s + 1 < NSEG) e = wait_word(rw, hb + ho_phys(HO_E), dead);
      if (lane < 4) lds4w(Y_M + lane * 1024, e);
      STAMP(h, 16 + s * 8 + 3); STAMPV(h, 16 + s * 8 + 7, first ? 0 : 1); STAMPV(h, 16 + s * 8, tpass);
      bar();
      next_pace();
      need_qk(s + 2 < NSEG ? s + 2 : NSEG - 1);
    }
  } else {
    const int rq = w - 1, tok = 16 * rq + n;
    const unsigned qvo = opaque((n >> 3) * 1024 + (g * 16 + 8 * (w - 1) + (n & 7)) * 16);
    v4u qn[6];
    load_qx(0, qn);
    for (int s = 0; s < NSEG; ++s) {
      const unsigned hb = ho_base(s, pol);
      v4u t[12];
      const int sn = s + 1 < NSEG ? s + 1 : s;
      const unsigned q0 = sopaque(qk_at(sn, h, 0, p0)), q1 = sopaque(qk_at(sn, h, 4, p0)), q2 = sopaque(qk_at(sn, h, 8, p0));
      proj(std::false_type{}, hb, t, [&]() {
#pragma unroll
        for (int u = 0; u < 6; ++u) lds16w(qxb + u * 1024, qn[u]); }, []() {}, [&](int u) { qn[u] = ld16c(rw, qvo + (u & 1) * 2048, u < 2 ? q0 : u < 4 ? q1 : q2); });
      __builtin_amdgcn_s_sleep(2);
      f4 d0, d1, d2;
      {
        v4u qf[6]; v2u kr[12];
#pragma unroll
        for (int u = 0; u < 6; ++u) { qf[u] = lds16(qxb + u * 1024); t[2 * u] = lds16(kxb + 2 * u * 1024); t[2 * u + 1] = lds16(kxb + (2 * u + 1) * 1024); }
#pragma unroll
        for (int u = 0; u < 6; ++u) {
          if (u == 0) { mfma_v0(d1, t[0], qf[0]); mfma_v0(d2, t[1], qf[0]); } else { mfma_v(d1, t[2 * u], qf[u]); mfma_v(d2, t[2 * u + 1], qf[u]); }
          kr[2 * u] = trread(imr + 2 * u * 256); kr[2 * u + 1] = trread(imr + (2 * u + 1) * 256);
          SB();
        }
#pragma unroll
        for (int u = 0; u < 6; ++u) {
          const v4u kf = {kr[2 * u].x, kr[2 * u].y, kr[2 * u + 1].x, kr[2 * u + 1].y};
          if (u == 0) mfma_v0(d0, kf, qf[0]); else mfma_v(d0, kf, qf[u]);
        }
        asm volatile("s_nop 7" : "+v"(d0), "+v"(d1), "+v"(d2));
      }
      bar();
      if (s + 1 < NSEG) {
        if (rq || g < 2) st16c(rw, lane * 16, px_at(s + 1, h * 2 + rq, p0), (v4u){SENT, SENT, SENT, SENT});
        st4c(rw, (h & 3) * 256 + lane * 4, px_at(s + 1, (32 + (h >> 2)) * 2 + rq, p0), SENT);
      }
      SB();
      next_pace();
      float v[10];
      const float NEG = -1.0e9f * 1.4426950408889634f;
#pragma unroll
      for (int i = 0; i < 4; ++i) { const int key = 16 * (g >> 1) + 4 * (g & 1) + i; v[i] = (key <= tok) ? d1[i] * cexp : NEG; v[4 + i] = (key + 8 <= tok) ? d2[i] * cexp : NEG; }
      v[8] = ((g & 2) ? d0[2] : d0[0]) * cexp; v[9] = ((g & 2) ? d0[3] : d0[1]) * cexp;
      float m = vmax3(vmax3(v[0], v[1], v[2]), vmax3(v[3], v[4], v[5]), vmax3(vmax3(v[6], v[7], v[8]), v[9], v[9]));
      m = xor32_max(xor16_max(m));
      float sum = 0.f;
#pragma unroll
      for (int i = 0; i < 10; ++i) { v[i] = __builtin_amdgcn_exp2f(v[i] - m); sum += v[i]; }
      sum = xor32_add(xor16_add(sum));
      const float rs = __builtin_amdgcn_rcpf(sum);
      v4u px;
#pragma unroll
      for (int i = 0; i < 4; ++i) px[i] = pk2a(v[i] * rs, v[4 + i] * rs);
      if (s == NSEG - 2) VMWAIT(0);
      if (rq || g < 2) st16c(rw, lane * 16, px_at(s, h * 2 + rq, p0), px);
      st4c(rw, ((h & 3) * 16 + n) * 16 + (2 * (g & 1) + (g >> 1)) * 4, px_at(s, (32 + (h >> 2)) * 2 + rq, p0), pk2a(v[8] * rs, v[9] * rs));
      STAMP(h, 16 + s * 8 + 4 + w);
    }
  }
  STAMP(h, 4);
  STAMPV(h, 3, (unsigned)__builtin_amdgcn_s_memtime());
}

template <bool PERM>
__device__ __forceinline__ void load_wo(const Params& p, int h, int lane, int w, v4u (&wo)[10][6]) {
  const int g = lane >> 4, r = (lane >> 2) & 3, cc = lane & 3;
  const unsigned lb = w * 30720;
  auto issue = [&](int u) {
#pragma unroll
    for (int i = 0; i < 10; ++i) {
      const int pos = i * 64 + lane, e = pos / 20, c16 = pos % 20;
      glds16(p.wo + (size_t)(h * E_ + u * 32 + e) * D_ + w * 160 + c16 * 8, lb + (u % 3) * 10240 + i * 1024);
    }
  };
  issue(0); issue(1); issue(2);
  const unsigned base = opaque(lb + (PERM ? 4 * g + r : 8 * g + r) * 320 + cc * 8);
#pragma unroll
  for (int u = 0; u < 6; ++u) {
    if (u <= 3) VMWAIT(20); else if (u == 4) VMWAIT(10); else VMWAIT(0);
#pragma unroll
    for (int t = 0; t < 10; ++t) {
      const unsigned a = base + (u % 3) * 10240 + t * 32;
      v2u lo = trread(a), hi = trread(a + (PERM ? 16 : 4) * 320);
      wo[t][u] = (v4u){lo.x, lo.y, hi.x, hi.y};
    }
    asm volatile("s_waitcnt lgkmcnt(0)" ::: "memory");
    if (u + 3 < 6) issue(u + 3);
  }
}

__device__ __forceinline__ void role_v2(const Params& p, unsigned char* ws, rsrc_t rw, int h, int lane, int w) {
  const int gg = lane >> 4, hi = lane >> 5, r = (lane >> 2) & 3, cc = lane & 3;
  v4u wo[5][12];
  {
    const unsigned lb = w * 30720;
    auto issue = [&](int u) {
#pragma unroll
      for (int i = 0; i < 10; ++i) {
        const int pos = i * 64 + lane, e = pos / 20, c16 = pos % 20;
        glds16(p.wo + (size_t)(h * E_ + u * 32 + e) * D_ + w * 160 + c16 * 8, lb + (u % 3) * 10240 + i * 1024);
      }
    };
    issue(0); issue(1); issue(2);
    const unsigned base = opaque(lb + (4 * hi + r) * 320 + (gg & 1) * 32 + cc * 8);
#pragma unroll
    for (int u = 0; u < 6; ++u) {
      if (u <= 3) VMWAIT(20); else if (u == 4) VMWAIT(10); else VMWAIT(0);
#pragma unroll
      for (int t = 0; t < 5; ++t)
#pragma unroll
        for (int k = 0; k < 2; ++k) { const unsigned a = base + (u % 3) * 10240 + t * 64 + k * 8 * 320; v2u lo = trread(a), hi2 = trread(a + 16 * 320); wo[t][2 * u + k] = (v4u){lo.x, lo.y, hi2.x, hi2.y}; }
      asm volatile("s_waitcnt lgkmcnt(0)" ::: "memory");
      if (u + 3 < 6) issue(u + 3);
    }
  }
#pragma unroll
  for (int t = 0; t < 5; ++t)
#pragma unroll
    for (int k = 0; k < 12; ++k) PIN_A(wo[t][k]);
  bar();
  bool dead = false;
  const int jf0 = C_WFLAG + 48 + (3 * h) / 4, jf1 = C_WFLAG + 48 + (3 * h + 2) / 4;
  const unsigned fo = ((lane & 1) ? jf1 : jf0) * 256u;
  int fl = -1;
  auto need = [&](int s) {
    for (unsigned sp = 0; !__all(fl >= s) && !dead; ++sp) {
      asm volatile("" ::: "memory");
      if (sp) __builtin_amdgcn_s_sleep(2);
      fl = (int)ld4c(rw, fo, WS_CTRL);
      if (sp > SPINCAP) { dead = true; st4c(rw, 0, WS_CTRL + C_TMO * 256, 2500 + s); }
    }
    __builtin_amdgcn_fence(__ATOMIC_ACQUIRE, "wavefront");
  };
  const unsigned l16 = lane * 16u, vw = opaque(w * 3072 + l16), vr = opaque((gg & 1) * 1024 + hi * 256 + (lane & 15) * 16);
  const unsigned uo = (gg & 1) * 65536u + hi * 256 + (lane & 15) * 16;
  v4u vn[3], va[12], oa[10], ob[10];
  const unsigned p0 = stripe_word(rw, lane) >> 3;
  auto ldv = [&](int s, int i) { vn[i] = ld16c(rw, l16, qk_at(s, h, 3 * w + i, p0 ^ 1u)); };
  auto put = [&](const v4u (&o)[10], int q, int s) { st16c(rw, uo, ux_at(s, w * 10 + (q & ~1), h, p0) + (q & 1) * 512, o[q]); };
#pragma unroll
  for (int t = 0; t < 10; ++t) ob[t] = (v4u){0, 0, 0, 0};
  auto fetch = [&](int s) {
    need(s);
#pragma unroll
    for (int i = 0; i < 3; ++i) ldv(s, i);
#pragma unroll
    for (int i = 0; i < 3; ++i) lds16w(vw + i * 1024, vn[i]);
    bar();
#pragma unroll
    for (int k = 0; k < 12; ++k) va[k] = lds16(vr + (k >> 1) * 2048 + (k & 1) * 512);
    bar();
  };
  fetch(0);
  auto seg = [&](v4u (&out)[10], const v4u (&prev)[10], int s) {
    if (w == 0) { STAMP(200 + h, 16 + s); STAMPV(200 + h, 128 + s, (unsigned)__builtin_amdgcn_s_memtime()); }
    const int sn = s + 1 < NSEG ? s + 1 : s;
    f16v acc[2];
    auto pack = [&](int t, int q) { const f16v a = acc[t & 1]; const int o = 4 * q;
      out[2 * t + q] = (v4u){pk2(a[o], a[o + 8]), pk2(a[o + 1], a[o + 9]), pk2(a[o + 2], a[o + 10]), pk2(a[o + 3], a[o + 11])}; asm volatile("" : "+v"(out[2 * t + q])); };
    static_for<60>([&](auto GC) {
      constexpr int G = decltype(GC)::value, t = G / 12, k = G % 12;
      if constexpr (G == 42) { if (s >= V_EAGER) { need(sn); ldv(sn, 0); ldv(sn, 1); ldv(sn, 2); } }
      mfma_big<k == 0, false, true>(acc[t & 1], va[k], wo[t][k]);
      SB();
      if constexpr (t >= 1 && (k == 3 || k == 5)) pack(t - 1, k == 5);
      if constexpr (G < 30 && G % 3 == 0) { if (s > V_EAGER) put(prev, G / 3, s - 1); }
      if constexpr (G == 2) { if (!__all(fl >= sn)) fl = (int)ld4c(rw, fo, WS_CTRL); }
      SB();
    });
    VMWAIT(0);
#pragma unroll
    for (int i = 0; i < 3; ++i) lds16w(vw + (sn & 1) * 12288 + i * 1024, vn[i]);
    bar();
    if (w == 0 && lane == 0 && s > V_EAGER) st4c(rw, 0, WS_CTRL + (C_UFLAG + h) * 256, (unsigned)(s - 1));
#pragma unroll
    for (int k = 0; k < 12; ++k) va[k] = lds16(vr + (sn & 1) * 12288 + (k >> 1) * 2048 + (k & 1) * 512);
    PAD_BIG();
    pack(4, 0); pack(4, 1);
    if (s < V_EAGER || s == NSEG - 1) {
#pragma unroll
      for (int t = 0; t < 10; ++t) put(out, t, s);
      VMWAIT(0);
      bar();
      if (w == 0 && lane == 0) st4c(rw, 0, WS_CTRL + (C_UFLAG + h) * 256, (unsigned)s);
      if (s < V_EAGER) fetch(s + 1);
    }
  };
  for (int s = 0; s < NSEG; s += 2) { seg(oa, ob, s); seg(ob, oa, s + 1); }
}

constexpr unsigned V_IMG = 122880, V_M = V_IMG + 3072, V_T = V_M + 10240;
__device__ __forceinline__ void role_v(const Params& p, unsigned char* ws, rsrc_t rw, int h, int lane, int w) {
  const int g = lane >> 4, n = lane & 15, r = (lane >> 2) & 3, cc = lane & 3;
  bool dead = false;
  v4u wo[10][6];
  load_wo<false>(p, h, lane, w, wo);
  v8u w2[10][3];
#pragma unroll
  for (int t = 0; t < 10; ++t)
#pragma unroll
    for (int k = 0; k < 3; ++k) { const v4u lo = wo[t][2 * k], hi = wo[t][2 * k + 1]; w2[t][k] = (v8u){lo.x, lo.y, lo.z, lo.w, hi.x, hi.y, hi.z, hi.w}; PIN_A(w2[t][k]); }
  bar();
  unsigned soff[6], own[6];
  blk_src(h, lane, w, soff, own);
  auto issue = [&](int i) {
#pragma unroll
    for (int q = 0; q < 6; ++q) glds16(p.wv + soff[q] + (unsigned)slot_ks(i < 5 ? 2 * i + 1 : 2 * (i - 5), own[q]) * (64u * HE_), (i % 5) * 24576 + (w * 6 + q) * 1024);
  };
  unsigned tb[3], tb2[3];
  { const int f = (r >> 1) | ((g & 1) << 1);
#pragma unroll
    for (int t = 0; t < 3; ++t) { tb[t] = opaque((8 * g + r) * 384 + (((3 * w + t) ^ f) * 32) + cc * 8); tb2[t] = opaque(tb[t] + 3 * 24576); } }
  auto rdq = [&](int slot, int t, int q) -> v2u {
    return trread((slot < 3 ? tb[t] + slot * 24576 : tb2[t] + (slot - 3) * 24576) + (32 * (q >> 1) + 4 * (q & 1)) * 384); };
  auto cat = [](const v2u (&q)[4]) -> v8u { return (v8u){q[0].x, q[0].y, q[1].x, q[1].y, q[2].x, q[2].y, q[3].x, q[3].y}; };
#pragma unroll
  for (int i = 0; i < 5; ++i) issue(i);
  v8u w1[5][3];
#pragma unroll
  for (int i = 0; i < 5; ++i) {
    VMWAIT(24);
    bar();
#pragma unroll
    for (int t = 0; t < 3; ++t) { v2u q[4];
#pragma unroll
      for (int k = 0; k < 4; ++k) q[k] = rdq(i, t, k);
      w1[i][t] = cat(q); }
    bar();
    if (i == 0) { PIN_A(w1[0][0]); PIN_A(w1[0][1]); }
    issue(i + 5);
  }
  VMWAIT(0);
  const unsigned pol = stripe_word(rw, lane);
  wait_ge(rw, C_SET1, NSETUP - 1, dead);
  bar();
  const int sidx = sparse_idx(lane);
  const unsigned imw = opaque(V_IMG + (4 * g + 2 * ((n >> 1) & 1)) * 16 + (n >> 2) * 4 + (n & 1) * 2 + 3 * w * 256), imr = opaque(V_IMG + lane * 16);
  const unsigned mw = opaque(V_M + w * 1024 + lane * 16), mw2 = opaque(V_M + 8192 + w * 512 + lane * 8);
  unsigned mr[4];
#pragma unroll
  for (int i = 1; i < 4; ++i) mr[i] = opaque(V_M + ((w + i) & 3) * 1024 + lane * 16);
  mr[0] = opaque(V_M + 8192 + lane * 16);
  const unsigned umo = h * 256 + n * 16 + g * 16384, umo2 = g >= 2 ? umo : 0xF0000000u;
  STAMP(32 + h, 1);
  unsigned tpass = rclk();
  for (int s = 0; s < NSEG; ++s) {
    const unsigned hb = ho_base(s, pol);
    bool first; unsigned cy[8] = {0, 0, 0, 0, 0, 0, 0, 0}; (void)cy;
    const unsigned ub = sopaque(WS_UM + (unsigned)((s & 1) * 40 + w * 10 - 2) * 16384u + (((pol >> 2) & 1) << 13));
    f4 ac2[10]; unsigned c0;
    {
      v4u a[10]; v2u a2; f4 acc[3]; v2u sa[3][4], sb[3][4], sc[3][4];
      auto zero = [&]() {
#pragma unroll
        for (int t = 0; t < 3; ++t) { acc[t] = (f4){0, 0, 0, 0}; asm volatile("" : "+v"(acc[t])); } };
      auto mm = [&](auto GC) {
        constexpr int G = decltype(GC)::value, i = G / 3, t = G % 3, j = i < 6 ? i : (i == 6 ? 7 : i == 7 ? 9 : i == 8 ? 6 : 8);
        if constexpr (j & 1) acc[t] = smfmac64(a[j], w1[j >> 1][t], acc[t], sidx);
        else acc[t] = smfmac64(a[j], (j == 0 || j == 6) ? cat(sa[t]) : (j == 4 ? cat(sc[t]) : cat(sb[t])), acc[t], sidx);
        SB();
      };
      const MemGather mg(s, h & 3, pol >> 3, w);
      SB();
      wait_until(tpass);
      CYC(cy[0]);
      SB();
      mg.load(rw, lane, a[0], a[1], a2);
      c0 = cclk();
#pragma unroll
      for (int i = 0; i < 12; ++i) { sa[i >> 2][i & 3] = rdq(0, i >> 2, i & 3); sb[i >> 2][i & 3] = rdq(1, i >> 2, i & 3); sc[i >> 2][i & 3] = rdq(2, i >> 2, i & 3); }
      zero();
      SB();
      lds16w(mw, a[0]); SB();
      lds16w(mw + 4096, a[1]); SB();
      *(LAS v2u*)mw2 = a2; SB();
      mm(std::integral_constant<int, 0>{});
      first = !MemGather::bad(a[0], a[1], a2);
      mm(std::integral_constant<int, 1>{});
      if (__builtin_expect(!first, 0)) {
        mg.late(rw, lane, a[0], a[1], a2, dead);
        lds16w(mw, a[0]); lds16w(mw + 4096, a[1]); *(LAS v2u*)mw2 = a2;
        zero();
        static_for<2>(mm);
      }
      CYC(cy[1]);
      bar();
      CYC(cy[2]);
      SB();
      static_for<28>([&](auto GC0) {
        constexpr int G = decltype(GC0)::value + 2;
        auto rda = [&](int j) -> v4u { return j < 8 ? lds16(mr[slot_rot(j)] + slot_grp(j) * 4096) : lds16(mr[0] + (j - 8) * 1024); };
        if constexpr (G == 2) { a[2] = rda(2); a[3] = rda(3); a[4] = rda(4); }
        else if constexpr (G < 8) a[G == 3 ? 5 : G == 4 ? 7 : G == 5 ? 9 : G == 6 ? 6 : 8] = rda(G == 3 ? 5 : G == 4 ? 7 : G == 5 ? 9 : G == 6 ? 6 : 8);
        else if constexpr (G < 16) { constexpr int i0 = (3 * (G - 8)) / 2, i1 = (3 * (G - 7)) / 2;
          sa[i0 >> 2][i0 & 3] = rdq(3, i0 >> 2, i0 & 3); if constexpr (i1 - i0 == 2) sa[(i0 + 1) >> 2][(i0 + 1) & 3] = rdq(3, (i0 + 1) >> 2, (i0 + 1) & 3); }
        else if constexpr (G < 24) { constexpr int i0 = (3 * (G - 16)) / 2, i1 = (3 * (G - 15)) / 2;
          sb[i0 >> 2][i0 & 3] = rdq(4, i0 >> 2, i0 & 3); if constexpr (i1 - i0 == 2) sb[(i0 + 1) >> 2][(i0 + 1) & 3] = rdq(4, (i0 + 1) >> 2, (i0 + 1) & 3); }
        SB();
        mm(std::integral_constant<int, G>{});
      });
      CYC(cy[3]);
#pragma unroll
      for (int t = 0; t < 3; ++t) { unsigned d = fold_pack(acc[t]); asm volatile("" : "+v"(d));
        *(LAS unsigned short*)(imw + t * 256) = (unsigned short)d; *(LAS unsigned short*)(imw + t * 256 + 16) = (unsigned short)(d >> 16); SB(); }
#pragma unroll
      for (int t = 0; t < 6; ++t) { ac2[t] = (f4){0, 0, 0, 0}; asm volatile("" : "+v"(ac2[t])); }
    }
    bar();
    CYC(cy[4]);
    {
      const v4u a0 = lds16(imr), a1 = lds16(imr + 1024), a2 = lds16(imr + 2048);
      SB();
#pragma unroll
      for (int t = 6; t < 10; ++t) { ac2[t] = (f4){0, 0, 0, 0}; asm volatile("" : "+v"(ac2[t])); }
      float fa = 0.f, fb = 0.f; unsigned f[12];
      f[0] = f[1] = 0u;
      asm volatile("" : "+v"(f[0]), "+v"(f[1]));
      SB();
      auto sw32 = [&](int i, int j2) { auto r = __builtin_amdgcn_permlane32_swap(f[i], f[j2], false, false); f[i] = r[0]; f[j2] = r[1]; asm volatile("" : "+v"(f[i]), "+v"(f[j2])); };
      auto sw16 = [&](int i, int j2) { auto r = __builtin_amdgcn_permlane16_swap(f[i], f[j2], false, false); f[i] = r[0]; f[j2] = r[1]; asm volatile("" : "+v"(f[i]), "+v"(f[j2])); };
      static_for<41>([&](auto GC) {
        constexpr int G = decltype(GC)::value, t = 2 * (G / 6) + (G & 1), k = (G % 6) >> 1;
        if constexpr (G < 30) ac2[t] = smfmac64(k == 0 ? a0 : k == 1 ? a1 : a2, w2[t < 10 ? t : 0][k], ac2[t], sidx);
        SB();
        if constexpr (G >= 7 && G < 37) {
          constexpr int tp = (G - 7) / 3, ph = (G - 7) % 3;
          if constexpr (ph == 0) { fa = ac2[tp][0] + ac2[tp][2]; asm volatile("" : "+v"(fa)); }
          if constexpr (ph == 1) { fb = ac2[tp][1] + ac2[tp][3]; asm volatile("" : "+v"(fb)); }
          if constexpr (ph == 2) { f[2 + tp] = pk2(fa, fb); asm volatile("" : "+v"(f[2 + tp])); }
        }
        if constexpr (G >= 13 && G % 6 == 1) sw16(2 * ((G - 7) / 6), 2 * ((G - 7) / 6) + 1);
        if constexpr (G >= 14 && G % 12 == 2) sw32(4 * (G / 12 - 1), 4 * (G / 12 - 1) + 2);
        if constexpr (G >= 15 && G % 12 == 3) sw32(4 * (G / 12 - 1) + 1, 4 * (G / 12 - 1) + 3);
        if constexpr (G >= 16 && G % 12 == 4) { constexpr int q = G / 12 - 1;
          st16c(rw, q == 0 ? umo2 : umo, ub + (unsigned)q * 65536u, (v4u){f[4 * q], f[4 * q + 1], f[4 * q + 2], f[4 * q + 3]}); }
        SB();
      });
    }
    if (w == 3 && lane == 0) report(rw, 32 + h, s, cclk() - c0 - 150u, pol >> 3);
    CYC(cy[5]);
    if (w == 3) { STAMP(32 + h, 16 + s * 8 + 3); STAMPV(32 + h, 16 + s * 8 + 7, first ? 0 : 1); }
    if (w == 0 && s + 1 < NSEG) {
      const unsigned e = wait_word(rw, hb + ho_phys(HO_E), dead);
      if (lane == 0) lds4w(V_T, e);
    }
    bar();
    { const unsigned e = __builtin_amdgcn_readfirstlane(lds4(V_T)); tpass = (e >> 8) + (e & 255u); }
  }
}

constexpr unsigned R_P = 0, R_E = 2048, R_C = 2064;
__device__ __forceinline__ void role_r(const Params& p, unsigned char* ws, rsrc_t rw, int c, int lane, int w) {
  const int g = lane >> 4, n = lane & 15;
  bool dead = false;
  const unsigned pol = stripe_word(rw, lane); unsigned hprev = 0;
  wait_ge(rw, C_SET1, NSETUP - 1, dead);
  const unsigned p0 = pol >> 3;
  const int sidx = sparse_idx(lane);
  const unsigned l16 = lane * 16u;
  const int fi = 2 * (lane >> 4) + ((lane >> 2) & 1), fq = 2 * (lane & 3) + ((lane >> 3) & 1);
  const unsigned so = g * 256 + n * 8;
  const unsigned prd = opaque(R_P + (fi & 1) * 256 + ((fi >> 1) * 16 + 2 * fq) * 4), pwr = opaque(R_P + w * 512 + lane * 4);
  const unsigned mo = lane * 4;
  const unsigned cadr = opaque(R_C + (lane ? 4 + (lane & 31) * 4 : 0)), cinc = opaque(lane ? 0u : 1u), crd = opaque(R_C);
  unsigned tbulk = rclk();
  Hint uxf(C_UFLAG + w * 8, 8, lane);
  if (w == 0 && lane == 0) lds4w(R_C, 0u);
  for (int s = 0; s + 1 < NSEG; ++s) {
    const unsigned hb = ho_base(s, pol);
    const unsigned umb = WS_UM + (unsigned)((s & 1) * 40 + c) * 16384u + (((pol >> 2) & 1) << 13);
    constexpr bool lastseg = false;
    uxf.need(rw, s, dead);
    wait_until(tbulk);
    unsigned cb0 = 0, cb1 = 0, cb2 = 0; (void)cb0; (void)cb1; (void)cb2;
    CYC(cb0);
    v8u ux[4];
#pragma unroll
    for (int k = 0; k < 4; ++k) {
      const unsigned o = ux_at(s, c, w * 8, p0) + 2 * k * 1024;
      const v4u lo = ld16c(rw, l16, o), hi = ld16c(rw, l16, o + 1024);
      ux[k] = (v8u){lo.x, lo.y, lo.z, lo.w, hi.x, hi.y, hi.z, hi.w};
    }
    if (!uxf.has(s + 1)) uxf.sample(rw);
    if (w == 0 && !lastseg) { wait_until(tbulk + 28); const unsigned e0 = wait_word(rw, hb + ho_phys(HO_E), dead); if (lane == 0) lds4w(R_E, e0); }
    VMWAIT(0);
    CYC(cb1);
    bar();
    CYC(cb2);
    const unsigned ew = __builtin_amdgcn_readfirstlane(lds4(R_E)), e = ew >> 8;
    unsigned tk1 = 0; (void)tk1;
    const unsigned pbase = lastseg ? hprev : hb;
    const unsigned pm = sopaque(pbase + ho_phys(HO_P + w * 1024)), pxo = sopaque(pbase + ho_phys(HO_P + 4096 + w * 4096));
    const unsigned hn = sopaque(mem_at(s + 1, 0, c * 256, p0)), hn1 = sopaque(mem_at(s + 1, 1, c * 256, p0)), hn2 = sopaque(mem_at(s + 1, 2, c * 256, p0)), hn3 = sopaque(mem_at(s + 1, 3, c * 256, p0));
    hprev = hb; tbulk = e + (ew & 255u) + PACE_UM;
    const unsigned umw = sopaque(umb + w * 2048);
    const unsigned ut = sopaque(umt_at(s, c, p0) + w * 2048);
    const unsigned full = sopaque(lastseg ? 0u : 4u * (unsigned)s + 4u);
    v8u u4; unsigned c0, sp = 0, why = 0, cy[8] = {0, 0, 0, 0, 0, 0, 0, 0}; (void)cy;
    {
      v4u pf[5]; f4 d;
      auto mul = [&]() -> bool {
#pragma unroll
        for (int f = 0; f < 4; ++f) { pf[f] = ld16c(rw, l16 + f * 1024, pxo); SB(); }
        const v2u x0 = __builtin_amdgcn_raw_buffer_load_b64(rw, so, pm, 16), x1 = __builtin_amdgcn_raw_buffer_load_b64(rw, so + 128, pm, 16);
        pf[4] = (v4u){x0.x, x0.y, x1.x, x1.y};
        SB();
        const v4u lo = ld16c(rw, l16, umw), hi = ld16c(rw, l16 + 1024, umw);
        u4 = (v8u){lo.x, lo.y, lo.z, lo.w, hi.x, hi.y, hi.z, hi.w};
        SB();
        d = (f4){0, 0, 0, 0};
        asm volatile("" : "+v"(d));
        SB();
#pragma unroll
        for (int f = 0; f < 4; ++f) d = smfmac64(pf[f], ux[f], d, sidx);
        d = smfmac64(pf[4], u4, d, sidx);
        CYC(cy[3]);
        const float s0 = d[0] + d[2], s1 = d[1] + d[3];
        lds4w(pwr, f2u(s0)); lds4w(pwr + 256, f2u(s1));
        SB();
        return __any(__builtin_isunordered(s0, s1));
      };
      if (!lastseg) wait_until(e);
      c0 = cclk();
      CYC(cy[0]);
      SB();
      if (__builtin_expect(mul(), 0)) {
        for (;; ++sp) {
          unsigned m = 0;
#pragma unroll
          for (int f = 0; f < 5; ++f) m |= or4(pf[f]);
          const unsigned m2 = max(max(max(u4[0], u4[1]), max(u4[2], u4[3])), max(max(u4[4], u4[5]), max(u4[6], u4[7])));
          if (sp == 0) why = (__all((m & 0x80008000u) == 0) ? 0 : 1) + (__all(m2 != SENT) ? 0 : 256);
          if (__all((m & 0x80008000u) == 0 && m2 != SENT) || dead) break;
          if (sp > SPINCAP) { dead = true; st4c(rw, 0, WS_CTRL + C_TMO * 256, 3000 + s); }
          asm volatile("" ::: "memory");
          mul();
        }
      }
    }
    CYC(cy[4]);
    {
      unsigned cnt; f2 t0, t1, t2, t3;
      asm volatile("ds_add_u32 %5, %6\n\tds_read_b32 %0, %8\n\t"
                   "ds_read_b64 %1, %7\n\tds_read_b64 %2, %7 offset:512\n\tds_read_b64 %3, %7 offset:1024\n\tds_read_b64 %4, %7 offset:1536\n\t"
                   "s_waitcnt lgkmcnt(0)"
                   : "=&v"(cnt), "=&v"(t0), "=&v"(t1), "=&v"(t2), "=&v"(t3) : "v"(cadr), "v"(cinc), "v"(prd), "v"(crd) : "memory");
      const f2 t = (t0 + t1) + (t2 + t3);
      const unsigned mv = pk2(t[0], t[1]), vo = cnt == full ? mo : 0xF0000000u;
      st4c(rw, vo, hn, mv); st4c(rw, vo, hn1, mv); st4c(rw, vo, hn2, mv); st4c(rw, vo, hn3, mv);
      SB();
      CYC(cy[6]);
    }
    bar();
    if (w == 0 && lane == 0) report(rw, 64 + c, s, cclk() - c0, p0);
    (void)why;
    STAMPV(64 + c, 16 + s * 8 + 4 + w, why | ((cy[3] - cy[0]) << 16));
    if (w == 2 && s < 40) { STAMPV(64 + c, 860 + s * 4, cb1 - cb0); STAMPV(64 + c, 861 + s * 4, cb2 - cb0); }
    st16c(rw, l16, ut, (v4u){u4[0], u4[1], u4[2], u4[3]}); st16c(rw, l16, ut + 1024, (v4u){u4[4], u4[5], u4[6], u4[7]});
    st16c(rw, l16, umw, (v4u){SENT, SENT, SENT, SENT}); st16c(rw, l16 + 1024, umw, (v4u){SENT, SENT, SENT, SENT});
    unsigned cb3 = 0, cb4 = 0; (void)cb3; (void)cb4;
    CYC(cb3);
    VMWAIT(0);
    bar();
    CYC(cb4);
    if (w == 2 && s < 40) STAMPV(64 + c, 862 + s * 4, cb4 - cb3);
    if (w == 3 && lane == 0) st4c(rw, 0, stripe(WS_CTRL + 65536, c * 128u, p0), (unsigned)s);
  }
}

__device__ __forceinline__ void role_t(const Params& p, unsigned char* ws, rsrc_t rw, int t, int lane, int w) {
  const int g = lane >> 4, n = lane & 15;
  bool dead = false;
  rsrc_t rx = mk_rsrc(p.x, S_ * D_ * 2);
  const unsigned l16 = lane * 16u, vm0 = g < 2 ? l16 : 0xF0000000u;
  const int cf = t + 20 * (w >> 1);
  unsigned p0 = 0;
  struct TB { v4u ux[16]; v2u xv; };
  TB A, B; v4u um[4], px[20];
  const unsigned pol = stripe_word(rw, lane);
  p0 = pol >> 3;
  const unsigned fo = (lane & 15) < 8 ? (C_UFLAG + w * 8 + (lane & 7)) * 256u : stripe(WS_CTRL + 65536, (t + 20 * (lane & 1)) * 128u, p0) - WS_CTRL;
  int fl = -1;
  auto need = [&](int s, int sr) {
    for (unsigned sp = 0; !__all(fl >= ((lane & 8) ? sr : s)) && !dead; ++sp) {
      asm volatile("" ::: "memory");
      if (sp) __builtin_amdgcn_s_sleep(2);
      fl = (int)ld4c(rw, fo, WS_CTRL);
      if (sp > SPINCAP) { dead = true; st4c(rw, 0, WS_CTRL + C_TMO * 256, 4500 + s); }
    }
    __builtin_amdgcn_fence(__ATOMIC_ACQUIRE, "wavefront");
  };
  auto ld_px = [&](int s, int i) {
    px[i] = i < 16 ? ld16c(rw, (i & 1) ? l16 : vm0, px_at(s, w * 16 + (i & 8), p0) + (i & 7) * 1024) : ld16c(rw, l16, px_at(s, 64 + w * 4, p0) + (i - 16) * 1024);
  };
  auto ld_early = [&](TB& b, int s, int k, unsigned vo) {
    if (k < 8) { b.ux[2 * (k < 8 ? k : 0)] = ld16c(rw, vo, ux_at(s, t, w * 8, p0) + k * 1024); b.ux[2 * (k < 8 ? k : 0) + 1] = ld16c(rw, vo, ux_at(s, t + 20, w * 8, p0) + k * 1024); }
    if (k == 8) b.xv = __builtin_amdgcn_raw_buffer_load_b64(rx, (unsigned)((s * 32 + (w & 1) * 16 + n) * D_ + 16 * cf + 4 * g) * 2, 0, 0);
  };
  const unsigned ho = (unsigned)(((2 * (cf & 1) + (g >> 1)) * 16 + n) * 16 + (g & 1) * 8), hb0 = WS_HF + (unsigned)((w & 1) * 20 + (cf >> 1)) * 1024u;
  wait_ge(rw, C_SET1, NSETUP - 1, dead);
  need(0, -1);
#pragma unroll
  for (int k = 0; k < 9; ++k) ld_early(A, 0, k, l16);
  auto seg = [&](int s, TB& cur, TB& nxt) {
    const int sn = s + 1 < NSEG ? s + 1 : s;
    unsigned cy[8] = {0, 0, 0, 0, 0, 0, 0, 0}; (void)cy;
    CYC(cy[0]);
    const bool lastseg = s + 1 == NSEG;
    if (!lastseg) need(sn, s);
    else { const unsigned e = wait_word(rw, ho_base(s - 1, pol) + ho_phys(HO_E), dead); wait_until((e >> 8) + (e & 255u) + 196u); }
    CYC(cy[1]);
    auto ld_um = [&](int i) { const int c = t + 20 * (i & 1);
      um[i] = ld16c(rw, l16, (lastseg ? WS_UM + (unsigned)((s & 1) * 40 + c) * 16384u + (((pol >> 2) & 1) << 13) : umt_at(s, c, p0)) + (w * 2 + (i >> 1)) * 1024); };
#pragma unroll
    for (int i = 0; i < 20; ++i) ld_px(s, i);
#pragma unroll
    for (int i = 0; i < 4; ++i) ld_um(i);
    SB();
    const unsigned red = opaque((s & 1) * 16384 + l16), le = s + 1 < NSEG ? l16 : 0xF0000000u;
    f4 acc[4];
    auto mul = [&](auto PRE) {
#pragma unroll
      for (int i = 0; i < 4; ++i) acc[i] = (f4){0, 0, 0, 0};
#pragma unroll
      for (int k = 0; k < 10; ++k) {
#pragma unroll
        for (int i = 0; i < 4; ++i) acc[i] = mfma32(k < 8 ? cur.ux[k < 8 ? 2 * k + (i >> 1) : 0] : um[k < 8 ? 0 : 2 * (k - 8) + (i >> 1)], px[2 * k + (i & 1)], acc[i]);
        if (decltype(PRE)::value && k < 5) ld_early(nxt, sn, k == 4 ? 8 : k, le);
        SB();
      }
    };
    mul(std::true_type{});
    CYC(cy[2]);
    const f4 chk = (acc[0] + acc[1]) + acc[2];
    if (__builtin_expect(__any(__builtin_isunordered(chk[0] + chk[1], chk[2] + chk[3])), 0)) {
      for (unsigned sp = 0;; ++sp) {
        asm volatile("" ::: "memory");
        unsigned m = 0;
#pragma unroll
        for (int i = 0; i < 20; ++i) ld_px(s, i);
#pragma unroll
        for (int i = 0; i < 4; ++i) ld_um(i);
#pragma unroll
        for (int i = 0; i < 20; ++i) m |= or4(px[i]);
        if (__all((m & 0x80008000u) == 0 && max(max(mx4(um[0]), mx4(um[1])), max(mx4(um[2]), mx4(um[3]))) != SENT) || dead) break;
        if (sp > SPINCAP) { dead = true; st4c(rw, 0, WS_CTRL + C_TMO * 256, 4000 + s); }
      }
      mul(std::false_type{});
    }
#pragma unroll
    for (int i = 0; i < 4; ++i) lds16w(red + (w * 4 + i) * 1024, __builtin_bit_cast(v4u, acc[i]));
    SB();
    CYC(cy[3]);
    fl = (int)ld4c(rw, fo, WS_CTRL);
    bar();
    CYC(cy[4]);
    const unsigned rr = red + w * 1024;
    const f4 o = (__builtin_bit_cast(f4, lds16(rr)) + __builtin_bit_cast(f4, lds16(rr + 4096))) + (__builtin_bit_cast(f4, lds16(rr + 8192)) + __builtin_bit_cast(f4, lds16(rr + 12288)));
    const v2u xv = cur.xv;
    const v2u hv = {nosent(pk2(bflo(xv.x) + rbf(o[0]), bfhi(xv.x) + rbf(o[1]))), nosent(pk2(bflo(xv.y) + rbf(o[2]), bfhi(xv.y) + rbf(o[3])))};
    st8c(rw, ho, hb0 + (unsigned)s * 40960u, hv);
    if (w == 3 && lane == 0) st4c(rw, 0, WS_CTRL + (C_TFLAG + t) * 256, (unsigned)s);
    CYC(cy[5]);
    if (w == 0) STAMP(104 + t, 16 + s);
    if (w == 0 && t == 0) MARK(0, s);
    SB();
#pragma unroll
    for (int k = 4; k < 8; ++k) ld_early(nxt, sn, k, le);
    CYC(cy[6]);
  };
  for (int s = 0; s < NSEG; s += 2) { seg(s, A, B); seg(s + 1, B, A); }
}

__device__ __forceinline__ v2u ld8c(rsrc_t r, unsigned v, unsigned s) { return __builtin_amdgcn_raw_buffer_load_b64(r, v, s, 16); }

template <int NS, int NK>
__device__ __forceinline__ void load_wmix(const unsigned short* W, int N, int row0, int sl, int lane, int w, v4u (&wt)[NS][NK]) {
  constexpr int NB = NK / 4;
  const int gg = lane >> 4, hi = lane >> 5, r = (lane >> 2) & 3, cc = lane & 3;
  const int sl0 = min(sl, N / 32 - 2);
  const unsigned short* src = W + (size_t)(row0 + (lane >> 3)) * N + sl0 * 32 + (lane & 7) * 8;
  const unsigned lb = w * 40960;
  auto issue = [&](int blk) {
#pragma unroll
    for (int i = 0; i < 8; ++i) glds16(src + (size_t)(blk * 64 + i * 8) * N, lb + (blk % 5) * 8192 + i * 1024);
  };
#pragma unroll
  for (int b = 0; b < 5; ++b) issue(b);
  const unsigned base = opaque(lb + (8 * hi + r) * 128 + (sl - sl0) * 64 + (gg & 1) * 32 + (2 * (cc & 1) + (cc >> 1)) * 8);
  static_for<NB>([&](auto BC) {
    constexpr int b = decltype(BC)::value, left = NB - 1 - b < 4 ? NB - 1 - b : 4;
    if constexpr (left == 4) VMWAIT(32); else if constexpr (left == 3) VMWAIT(24); else if constexpr (left == 2) VMWAIT(16); else if constexpr (left == 1) VMWAIT(8); else VMWAIT(0);
#pragma unroll
    for (int i = 0; i < NS; ++i)
#pragma unroll
      for (int k = 0; k < 4; ++k) { const unsigned a = base + (b % 5) * 8192 + k * 2048 + i * 64; v2u lo = trread(a), hi2 = trread(a + 512); wt[i][4 * b + k] = (v4u){lo.x, lo.y, hi2.x, hi2.y}; }
    asm volatile("s_waitcnt lgkmcnt(0)" ::: "memory");
    if constexpr (b + 5 < NB) issue(b + 5);
  });
#pragma unroll
  for (int i = 0; i < NS; ++i)
#pragma unroll
    for (int k = 0; k < NK; ++k) if (k * NS < 64) PIN_A(wt[i][k]);
}

constexpr unsigned M_ST = 81920, M_CS = M_ST + 2048;
template <int STAGE, int NS, int NPROD, bool JIT>
__device__ __forceinline__ void role_mix(const Params& p, unsigned char* ws, rsrc_t rw, int j, int S0, int S1, unsigned MYFLAG, unsigned INFLAG, int lane, int w) {
  constexpr int STEP = JIT ? 1 : 2;
  int par = 1;
  constexpr int OKS = STAGE == 3 ? 48 : 20, NG = 40 * NS;
  constexpr unsigned IN = STAGE == 1 ? WS_HF : STAGE == 2 ? WS_H1F : WS_X1F, OUT = STAGE == 1 ? WS_H1F - 20480u : STAGE == 2 ? WS_X1F : WS_H2F;
  const int g = lane >> 4, n = lane & 15, hi = lane >> 5, tok = lane & 31;
  const unsigned l16 = opaque(lane * 16u), OOB = 0xF0000000u;
  bool dead = false;
  const int sl = (j * 4 + w) * NS;
  unsigned vo[NS];
#pragma unroll
  for (int i = 0; i < NS; ++i) {
    const int q = (sl + i) / 5, tt = STAGE == 1 ? tok + 16 + (q == 0 ? 1 : q == 1 ? -1 : q == 2 ? 2 : -2) : tok;
    vo[i] = (unsigned)((tt >> 4) * (OKS * 1024) + (sl + i) * 1024 + (hi * 16 + (tt & 15)) * 16);
  }
  auto put = [&](int s, int c, v4u o) { st16c(rw, vo[c >> 1], OUT + (unsigned)s * (OKS * 2048u) + (c & 1) * 512, o); };
  const bool stat_writer = STAGE == 1 && j == 0 && w == 0;
  const unsigned sto = hi == 0 ? tok * 8u : OOB;
  if (JIT) wait_ge(rw, C_WFLAG + 20 + STAGE * 8 + j, 0, dead);
  v4u wt[NS][40];
  load_wmix<NS, 40>(STAGE == 1 ? p.w1 : STAGE == 2 ? p.w2 : p.w3, STAGE == 3 ? F_ : D_, 0, sl, lane, w, wt);
  v4u ones = {0x3F803F80u, 0x3F803F80u, 0x3F803F80u, 0x3F803F80u};
  asm volatile("s_nop 1" : "+v"(ones));
  bar();
  const unsigned csa = opaque(M_CS + w * 512 + hi * 256);
  f4 cu[2] = {{0, 0, 0, 0}, {0, 0, 0, 0}};
  if (STAGE != 2) {
    f16v cs[NS];
    static_for<NG>([&](auto GC) { constexpr int G = decltype(GC)::value, k = G / NS, i = G % NS; mfma_big<k == 0, (k * NS < 64), false>(cs[i], wt[i][k], ones); });
    PAD_BIG();
#pragma unroll
    for (int q = 0; q < 4 * NS; ++q) { const f16v a = cs[q >> 2]; const int o = 4 * (q & 3); lds16w(csa + q * 16, (v4u){f2u(a[o]), f2u(a[o + 1]), f2u(a[o + 2]), f2u(a[o + 3])}); }
    cu[0] = __builtin_bit_cast(f4, lds16(csa)); cu[1] = __builtin_bit_cast(f4, lds16(csa + 16));
  }
  auto arm = [&](int s, int c) {
    st16c(rw, l16, OUT + (STAGE == 1 ? 20480u : 0u) + (unsigned)((s * 2 + (c & 1)) * OKS + sl + (c >> 1)) * 1024u, (v4u){SENT, SENT, SENT, SENT});
    if (stat_writer && c == 0) st8c(rw, sto, WS_ST + (unsigned)s * 256u, (v2u){SENT, SENT});
    if (STAGE == 2 && (c & 1)) st8c(rw, sto, WS_XST + (unsigned)(s * 20 + sl + (c >> 1)) * 256u, (v2u){SENT, SENT});
  };
  const int SA = STAGE == 1 ? S1 - 1 : S1;
  if (STAGE != 1 || JIT) {
    for (int s = S0; s < (JIT ? ARM + (STAGE == 1 ? NSEG - SA : 0) : S1); s += STEP) {
#pragma unroll
      for (int c = 0; c < 2 * NS; ++c) arm(!JIT || s < ARM ? s : SA + s - ARM, c);
    }
  } else wait_ge(rw, C_S1FLAG + j, 0, dead);
  if (STAGE == 1 && JIT) {
    VMWAIT(0);
#pragma unroll
    for (int c = 0; c < 2 * NS; ++c) { put(-1, c, (v4u){0u, 0u, 0u, 0u}); put(NSEG, c, (v4u){0u, 0u, 0u, 0u}); }
  }
  VMWAIT(0);
  bar();
  Hint hint(INFLAG, NPROD, lane);
  auto want = [&](int s) { return STAGE == 2 ? min(s + 1, NSEG - 1) : s; };
  f4 s1[2], s2[2];
  auto lda = [&](int s, int q) { return ld16c(rw, l16 + ((q >> 1) & 3) * 1024, IN + (unsigned)s * 40960u + w * 5120 + (q & 1) * 20480 + (q >> 3) * 4096); };
  unsigned inmax = 0; v2u xs[3];
  auto ldxs = [&](int s, int i) { xs[i] = ld8c(rw, i < 2 ? hi * 256u + tok * 8u : sto, WS_XST + (unsigned)(s * 20 + 5 * w + 2 * i) * 256u); };
  auto in_piece = [&](unsigned dst, int q, v4u a) {
    if (STAGE == 3) { inmax = max(q ? inmax : 0u, mx4(a)); asm volatile("" : "+v"(inmax)); }
    else if (q < 2) mfma_v0(s1[q & 1], ones, a); else mfma_v(s1[q & 1], ones, a);
    if (STAGE == 1) { if (q < 2) mfma_v0(s2[q & 1], a, a); else mfma_v(s2[q & 1], a, a); }
    lds16w(dst + q * 1024, a);
    if (STAGE != 3 && q == 9) asm volatile("s_nop 7");
    SB();
  };
  auto suspect = [&]() -> bool {
    if (STAGE == 3) return __any(max(max(inmax, max(xs[0].x, xs[0].y)), max(max(xs[1].x, xs[1].y), max(xs[2].x, xs[2].y))) == SENT);
    return __any(__builtin_isunordered(s1[0][0], s1[1][0]));
  };
  auto in_all = [&](int s, unsigned dst) -> bool {
    v4u a[10]; unsigned m = 0;
#pragma unroll
    for (int q = 0; q < 10; ++q) a[q] = lda(s, q);
    if (STAGE == 3) { ldxs(s, 0); ldxs(s, 1); ldxs(s, 2); }
#pragma unroll
    for (int q = 0; q < 10; ++q) in_piece(dst, q, a[q]);
    if (__builtin_expect(!suspect(), 1)) return true;
    if (STAGE == 3) return false;
#pragma unroll
    for (int q = 0; q < 10; ++q) m = max(m, mx4(a[q]));
    return __all(m != SENT);
  };
  auto in_finish = [&](int s, unsigned dst, bool again) {
    if (again) {
      for (unsigned sp = 0;; ++sp) {
        asm volatile("" ::: "memory");
        if (in_all(s, dst) || dead) break;
        if (sp > SPINCAP) { dead = true; st4c(rw, 0, WS_CTRL + C_TMO * 256, 7100 + s); }
      }
    }
    if (STAGE == 1) {
      const float d0 = (n & 2) ? ((n & 1) ? s2[0][3] : s2[0][2]) : ((n & 1) ? s2[0][1] : s2[0][0]);
      const float d1 = (n & 2) ? ((n & 1) ? s2[1][3] : s2[1][2]) : ((n & 1) ? s2[1][1] : s2[1][0]);
      if (g == (n >> 2)) lds16w(M_ST + (par ^ 1) * 1024 + w * 256 + n * 16, (v4u){f2u(s1[0][0]), f2u(d0), f2u(s1[1][0]), f2u(d1)});
    }
    if (STAGE == 3) {
      const float a = xor32_add(u2f(xs[0].x) + u2f(xs[1].x) + u2f(xs[2].x)), b = xor32_add(u2f(xs[0].y) + u2f(xs[1].y) + u2f(xs[2].y));
      *(LAS f2*)(M_ST + (par ^ 1) * 1024 + w * 256 + n * 16 + (g & 1) * 8) = (f2){a, b};
    }
  };
  const unsigned xo = (g & 1) * 1024 + hi * 256 + n * 16;
  auto xoff = [](int k) { return (k >> 1) * 2048 + (k & 1) * 512; };
  v4u xr[4]; f2 sp4[4];
  auto head = [&]() {
    par ^= 1;
    const unsigned src = par * 40960 + xo;
    xr[0] = lds16(src); xr[1] = lds16(src + xoff(1)); xr[2] = lds16(src + xoff(2));
    if (STAGE != 2) {
#pragma unroll
      for (int q = 0; q < 4; ++q) sp4[q] = *(LAS f2*)(M_ST + par * 1024 + q * 256 + n * 16 + (g & 1) * 8);
    }
    SB();
  };
  auto fetch = [&](int s) {
    hint.need(rw, want(s), dead);
    const unsigned dst = opaque((par ^ 1) * 40960 + w * 10240 + l16);
    in_finish(s, dst, true);
    bar();
    head();
  };
  float pv[16 * NS]; v4u phv[2 * NS]; float prs = 0.f, pnm = 0.f, pga = 0.f, pgb = 0.f, xa = 0.f, xb = 0.f, u[8]; v4u po;
#pragma unroll
  for (int i = 0; i < NS; ++i) { phv[2 * i] = phv[2 * i + 1] = ones; for (int e = 0; e < 16; ++e) pv[16 * i + e] = 0.f; }
  auto epi_two = [&](auto PC, float* y, int c, int e0) {
    constexpr int ph = decltype(PC)::value;
#pragma unroll
    for (int e = e0; e < e0 + 2; ++e) {
      if constexpr (STAGE != 2) {
        if constexpr (ph == 0) y[e] = __builtin_fmaf(pnm, cu[e >> 2][e & 3], y[e]);
        if constexpr (ph == 1) u[e] = y[e] * y[e];
        if constexpr (ph == 2) u[e] = __builtin_fmaf(u[e], pga, pgb);
        if constexpr (ph == 3) u[e] *= y[e];
        if constexpr (ph == 4) u[e] = __builtin_amdgcn_exp2f(u[e]);
        if constexpr (ph == 5) u[e] = __builtin_fmaf(u[e], prs, prs);
        if constexpr (ph == 6) u[e] = __builtin_amdgcn_rcpf(u[e]);
        if constexpr (ph == 7) y[e] *= u[e];
      } else {
        if constexpr (ph == 0) u[e] = (e & 1) ? bfhi(phv[c][e >> 1]) : bflo(phv[c][e >> 1]);
        if constexpr (ph == 1) u[e] = __builtin_fmaf(u[e], prs, pnm);
        if constexpr (ph == 2) y[e] += u[e];
        if constexpr (ph == 3) { xa = ((c & 1) || e) ? xa + y[e] : y[e]; xb = ((c & 1) || e) ? __builtin_fmaf(y[e], y[e], xb) : y[e] * y[e]; }
      }
      asm volatile("" : "+v"(u[e]), "+v"(y[e]));
    }
  };
  auto epi_step = [&](auto EC, int s) {
    constexpr int E = decltype(EC)::value, c = E / 20, gi = E % 20;
    float* y = pv + 8 * c;
    if constexpr (gi < 16) epi_two(std::integral_constant<int, (gi >> 1)>{}, y, c, 2 * (gi & 1));
    if constexpr (gi >= 2 && gi < 18) epi_two(std::integral_constant<int, ((gi - 2) >> 1)>{}, y, c, 4 + 2 * (gi & 1));
    if constexpr (STAGE != 2 && (gi == 16 || gi == 17)) cu[gi & 1] = __builtin_bit_cast(f4, lds16(csa + ((c + 1) % (2 * NS)) * 32 + (gi & 1) * 16));
    if constexpr (STAGE == 2 && (c & 1) && gi == 12) st8c(rw, sto, WS_XST + (unsigned)(s * 20 + sl + (c >> 1)) * 256u, (v2u){nosent(f2u(xor32_add(xa))), nosent(f2u(xor32_add(xb)))});
    if constexpr (gi == 18) { po = (v4u){pk2(y[0], y[1]), pk2(y[2], y[3]), pk2(y[4], y[5]), pk2(y[6], y[7])}; asm volatile("" : "+v"(po)); }
    if constexpr (gi == 19) {
      put(s, c, STAGE == 2 ? (v4u){nosent(po.x), nosent(po.y), nosent(po.z), nosent(po.w)} : po);
      if constexpr (E == NG - 1) {
        if (w == 3 && lane == 0) st4c(rw, 0, WS_CTRL + (MYFLAG + j) * 256, (unsigned)s);
        if (w == 0) STAMP(232 + (STAGE - 1) * 4 + (j & 3), 16 + s);
        if (w == 0 && j == (STAGE == 1 ? 4 : 0)) MARK(STAGE, s);
      }
    }
  };
#pragma unroll
  for (int e = 0; e < 8; ++e) u[e] = 0.f;
  bool pending = false;
  fetch(S0);
  for (int s = S0; s < S1; s += STEP) {
    const int sn = s + STEP;
    unsigned cy[8] = {0, 0, 0, 0, 0, 0, 0, 0}; (void)cy;
    CYC(cy[0]);
    const bool ready = NS == 1 ? (JIT && sn + 1 < S1 && hint.has(want(sn) + 1)) : (sn < S1 && hint.has(want(sn)));
    __builtin_amdgcn_fence(__ATOMIC_ACQUIRE, "wavefront");
    const unsigned src = opaque(par * 40960 + xo), dst = opaque((par ^ 1) * 40960 + w * 10240 + l16);
    f16v acc[NS]; v4u hv[2 * NS]; v2u stv = {0, 0}; float mu = 0.f, rstd = 0.f;
    if (STAGE != 2) {
      const f2 st = (sp4[0] + sp4[1]) + (sp4[2] + sp4[3]);
      mu = st[0] * (1.f / D_);
      const float var = fmaxf(st[1] * (1.f / D_) - mu * mu, 0.f);
      rstd = __builtin_amdgcn_rcpf(rbf(__builtin_amdgcn_sqrtf(rbf(rbf(var) + 1e-5f))));
      if (stat_writer) st8c(rw, sto, WS_ST + (unsigned)s * 256u, (v2u){nosent(f2u(rbf(mu))), nosent(f2u(rstd))});
      if (STAGE == 1) rstd *= __builtin_amdgcn_rcpf(rbf(__builtin_amdgcn_sqrtf(rbf(rbf(var * rstd * rstd) + 1e-5f))));
      else mu = rbf(mu);
    }
    SB();
    auto body = [&](auto PRE, auto EPI) {
      constexpr int AB = NS == 1 ? 10 : 5, L0 = 0, LS = NS == 1 ? 1 : 2, WS0 = NS == 1 ? 18 : 10, WSS = 2;
      v4u an[AB];
      static_for<NG>([&](auto GC) {
        constexpr int G = decltype(GC)::value, k = G / NS, i = G % NS;
        mfma_big<k == 0, (k * NS < 64), false>(acc[i], wt[i][k], xr[k & 3]);
        SB();
        if constexpr (i == 0 && k < 37) xr[(k + 3) & 3] = lds16(src + xoff(k + 3));
        if constexpr (i == NS - 1) {
          if constexpr (JIT && k == 40 - 2 * NS) VMWAIT(0);
          if constexpr (JIT && k >= 40 - 2 * NS) arm(s + ARM < SA ? s + ARM : NSEG + 2, k - (40 - 2 * NS));
          if constexpr (decltype(PRE)::value && STAGE == 3 && k == 33) asm volatile("" : "+v"(xs[0]), "+v"(xs[1]), "+v"(xs[2]));
          if constexpr (STAGE == 2 && k == 36) asm volatile("" : "+v"(stv), "+v"(hv[0]), "+v"(hv[2 * NS - 1]));
          if constexpr (JIT && k == 39 - 2 * NS) asm volatile("" : "+v"(hint.v));
          if constexpr (decltype(PRE)::value && k >= L0 && (k - L0) % LS == 0 && (k - L0) / LS < 10) an[((k - L0) / LS) % AB] = lda(sn, (k - L0) / LS);
          if constexpr (k == (NS == 1 ? 16 : 25)) hint.sample(rw);
          if constexpr (decltype(PRE)::value && STAGE == 3 && (k == 17 || k == 21 || k == 23)) ldxs(sn, k == 17 ? 0 : k == 21 ? 1 : 2);
          if constexpr (STAGE == 2 && k == 11) stv = ld8c(rw, tok * 8u, WS_ST + (unsigned)s * 256u);
          if constexpr (STAGE == 2 && k >= 12 && k < 12 + 2 * NS) hv[k - 12] = ld16c(rw, vo[(k - 12) >> 1], WS_HF + (unsigned)s * 40960u + ((k - 12) & 1) * 512);
        }
        if constexpr (decltype(EPI)::value) epi_step(GC, s - 1);
        SB();
        if constexpr (decltype(PRE)::value && i == 0 && k >= WS0 && (k - WS0) % WSS == 0 && (k - WS0) / WSS < 10) in_piece(dst, (k - WS0) / WSS, an[((k - WS0) / WSS) % AB]);
      });
      PAD_BIG();
      if (STAGE == 2) {
        for (unsigned sp = 0; __any(max(stv.x, stv.y) == SENT) && !dead; ++sp) {
          asm volatile("" ::: "memory");
          stv = ld8c(rw, tok * 8u, WS_ST + (unsigned)s * 256u);
          if (sp > SPINCAP) { dead = true; st4c(rw, 0, WS_CTRL + C_TMO * 256, 7200 + s); }
        }
        mu = u2f(stv.x); rstd = u2f(stv.y);
#pragma unroll
        for (int c = 0; c < 2 * NS; ++c) phv[c] = hv[c];
      }
#pragma unroll
      for (int e = 0; e < 16 * NS; ++e) pv[e] = acc[e >> 4][e & 15];
      if (STAGE == 2) { prs = rstd; pnm = -mu * rstd; }
      else {
        if (__builtin_isunordered(mu, rstd)) mu = rstd = u2f(0x7FC00000u);
        pnm = -mu; prs = __builtin_amdgcn_rcpf(rstd);
        pgb = -2.302208198144325f * rstd; pga = 0.044715f * rstd * rstd * pgb;
      }
    };
    if (ready) {
      CYC(cy[2]);
      if (pending) body(std::true_type{}, std::true_type{}); else body(std::true_type{}, std::false_type{});
      CYC(cy[3]);
      in_finish(sn, dst, __builtin_expect(suspect(), 0));
      bar();
      head();
      CYC(cy[4]);
      pending = true;
    } else {
      if (pending) body(std::false_type{}, std::true_type{}); else body(std::false_type{}, std::false_type{});
      pending = false;
      if (NS == 1 && sn < S1 && hint.has(want(sn))) {
        __builtin_amdgcn_fence(__ATOMIC_ACQUIRE, "wavefront");
        v4u a[10];
#pragma unroll
        for (int q = 0; q < 10; ++q) a[q] = lda(sn, q);
        if (STAGE == 3) { ldxs(sn, 0); ldxs(sn, 1); ldxs(sn, 2); }
        SB();
        static_for<NG>([&](auto EC) { epi_step(EC, s); SB(); });
#pragma unroll
        for (int q = 0; q < 10; ++q) in_piece(dst, q, a[q]);
        in_finish(sn, dst, __builtin_expect(suspect(), 0));
        bar();
        head();
      } else {
        static_for<NG>([&](auto EC) { epi_step(EC, s); SB(); });
        if (sn >= S1) break;
        fetch(sn);
#pragma unroll
        for (int e = 0; e < 16 * NS; ++e) pv[e] = 0.f;
      }
    }
  }
}

__device__ __forceinline__ void role_mix4(const Params& p, unsigned char* ws, rsrc_t rw, int j, int S0, int S1, unsigned INFLAG, int NPROD, int lane, int w) {
  const int g = lane >> 4, n = lane & 15, hi = lane >> 5;
  const unsigned l16 = lane * 16u;
  bool dead = false;
  wait_ge(rw, C_WFLAG + 56 + j, 0, dead);
  v4u wt[2][24];
  load_wmix<2, 24>(p.w4, D_, 384 * w, 2 * j, lane, w, wt);
  bar();
  Hint hint(INFLAG, NPROD, lane);
  const int slab = 2 * j + (w >> 1);
  const unsigned ao = (g & 1) * 49152u + hi * 256 + n * 16, fo = (g & 1) * 20480u + slab * 1024 + (w & 1) * 512 + hi * 256 + n * 16;
  v4u a[24];
  auto lda = [&](int s, int k, unsigned vo) { a[k] = ld16c(rw, vo, WS_H2F + (unsigned)(s * 96 + 12 * w + (k >> 1)) * 1024u + (k & 1) * 512); };
  const bool late = S0 > 0;
  hint.need(rw, S0, dead);
#pragma unroll
  for (int q = 0; q < 24; ++q) lda(S0, q, ao);
  int par = 0;
  for (int s = S0; s < S1; s += late ? 2 : 1) {
    const int sn = s + 1 < S1 ? s + 1 : s;
    if (!late) hint.need(rw, sn, dead);
    else if (s > S0) {
      hint.need(rw, s, dead);
#pragma unroll
      for (int q = 0; q < 24; ++q) lda(s, q, ao);
    }
    const unsigned aon = s + 1 < S1 && !late ? ao : 0xF0000000u;
    par ^= 1;
    const v4u hv = ld16c(rw, fo, WS_HF + (unsigned)s * 40960u), xv = ld16c(rw, fo, WS_X1F + (unsigned)s * 40960u);
    f16v acc[2];
    auto body = [&]() {
      static_for<24>([&](auto KC) {
        constexpr int k = decltype(KC)::value;
        mfma_big<k == 0, true, false>(acc[0], wt[0][k], a[k]);
        if constexpr (k == 8) hint.sample(rw);
        SB();
        mfma_big<k == 0, true, false>(acc[1], wt[1][k], a[k]);
        SB();
        lda(sn, k, aon);
        SB();
      });
      PAD_BIG();
    };
    body();
    if (__builtin_expect(__any(acc[0][0] != acc[0][0]), 0)) {
      for (unsigned sp = 0;; ++sp) {
        asm volatile("" ::: "memory");
        unsigned m = 0;
#pragma unroll
        for (int q = 0; q < 24; ++q) lda(s, q, ao);
#pragma unroll
        for (int q = 0; q < 24; ++q) m = max(m, mx4(a[q]));
        if (__all(m != SENT) || dead) break;
        if (sp > SPINCAP) { dead = true; st4c(rw, 0, WS_CTRL + C_TMO * 256, 7300 + s); }
      }
      body();
    }
    const unsigned red = opaque(par * 32768 + l16);
#pragma unroll
    for (int i = 0; i < 8; ++i) { const f16v d = acc[i >> 2]; const int o = 4 * (i & 3);
      lds16w(red + (w * 8 + i) * 1024, (v4u){f2u(d[o]), f2u(d[o + 1]), f2u(d[o + 2]), f2u(d[o + 3])}); }
    bar();
    f4 o[2];
#pragma unroll
    for (int tt = 0; tt < 2; ++tt) {
      const unsigned rr = red + (w * 2 + tt) * 1024;
      o[tt] = (__builtin_bit_cast(f4, lds16(rr)) + __builtin_bit_cast(f4, lds16(rr + 8192))) + (__builtin_bit_cast(f4, lds16(rr + 16384)) + __builtin_bit_cast(f4, lds16(rr + 24576)));
    }
    v4u r;
#pragma unroll
    for (int q = 0; q < 4; ++q)
      r[q] = pk2(bflo(hv[q]) + rbf(bflo(xv[q]) + rbf(o[q >> 1][2 * (q & 1)])), bfhi(hv[q]) + rbf(bfhi(xv[q]) + rbf(o[q >> 1][2 * (q & 1) + 1])));
    *(v4u*)(p.out + (size_t)(s * 32 + (lane & 31)) * D_ + slab * 32 + (w & 1) * 16 + hi * 8) = r;
    if (w == 0) STAMP(244 + (j & 3), 16 + s);
    if (w == 0 && j == 0) MARK(4, s);
  }
}

__global__ void __launch_bounds__(256, 1) k_main(Params p) {
  extern __shared__ __attribute__((aligned(16))) unsigned char lds_raw[];
  unsigned char* ws = ws_base(p.ws);
  rsrc_t rw = mk_rsrc(ws, WS_END);
  const int tid = threadIdx.x, w = __builtin_amdgcn_readfirstlane(tid >> 6);
  if (tid == 0) {
    const unsigned xcc = (unsigned)__builtin_amdgcn_s_getreg(20 | (3 << 11)) & 7u;
    unsigned v = 0;
    for (unsigned k = 0; k < 8; ++k) {
      const unsigned x = (xcc + k) & 7u;
      const unsigned rk = __hip_atomic_fetch_add((unsigned*)(ws + WS_CTRL + (C_TICKET + x) * 256), 1u, __ATOMIC_RELAXED, __HIP_MEMORY_SCOPE_AGENT) + 1u;
      if (rk < 32u) { v = rk * 8 + x; break; }
    }
    lds4w(0, v);
  }
  bar();
  const int v = __builtin_amdgcn_readfirstlane(lds4(0));
  bar();
  const int x = v & 7, idx = (v >> 3) * 4 + (x & 3);
  if (x < 4 ? idx >= 64 : idx >= 104) setup_job(p, ws, rw, x < 4 ? idx - 64 : idx - 40, lane_id(), w);
  int s1 = -1, s4 = -1, s40 = 0, s41 = S3_SPLIT, s4n = 6; unsigned s4f = C_S3FLAG;
  if (x < 4) {
    if (idx < 32) role_y(p, ws, rw, idx, lane_id(), w);
    else if (idx < 64) role_v(p, ws, rw, idx - 32, lane_id(), w);
    else if (idx < 104) role_r(p, ws, rw, idx - 64, lane_id(), w);
    else if (idx < 124) role_t(p, ws, rw, idx - 104, lane_id(), w);
    else if (idx == 124) {
      if (w > 0) return;
      const int lane = lane_id();
      const unsigned pol = stripe_word(rw, lane);
      bool dead = false;
      const unsigned t00 = rclk();
      unsigned tb = t00, cb = cclk(), tn = tb, cn = cb;
      wait_ge(rw, C_SET1, NSETUP - 1, dead);
      unsigned t = t00, esty = 2800, estr = 1500, c1 = 138, c3 = 74, la = 0, lb = 0; (void)la; (void)lb;
      for (int s = 0; s + 1 < NSEG; ++s) {
        const unsigned hb = ho_base(s, pol);
        wait_until(t);
        unsigned e = t + c1;
        for (unsigned sp = 0;; ++sp) {
          asm volatile("" ::: "memory");
          unsigned m = 0;
#pragma unroll
          for (int k = 0; k < 10; ++k) m = max(m, mx4(ld16c(rw, lane * 16, hb + ho_phys(HO_MEM + k * 1024))));
          if (__all(m != SENT) || dead) { if (sp) e = rclk() + c1 - PACE_LATE; break; }
          if (sp > SPINCAP) { dead = true; st4c(rw, 0, WS_CTRL + C_TMO * 256, 6000 + s); }
        }
        if (lane == 0) st4c(rw, 0, hb + ho_phys(HO_E), (e << 8) | c3);
        STAMPV(124, 32 + s, c1 * 256 + c3);
        t = e + c3;
        if (s == 1) { tb = tn = rclk(); cb = cn = cclk(); }
        if ((s & 3) == 1) { tb = tn; cb = cn; tn = rclk(); cn = cclk(); }
        if (s >= 2) {
          unsigned a = ld4c(rw, lane * 128, stripe(WS_REP, 0, pol >> 3)), b = ld4c(rw, lane * 128, stripe(WS_REP, 8192, pol >> 3));
          a = (a >> 16) == (unsigned)(s - 1) ? a & 65535u : 0u; b = ((b >> 16) == (unsigned)(s - 1) && lane < 40) ? b & 65535u : 0u;
#pragma unroll
          for (int o = 1; o < 64; o <<= 1) { a = max(a, (unsigned)__shfl_xor((int)a, o)); b = max(b, (unsigned)__shfl_xor((int)b, o)); }
          a = __builtin_amdgcn_readfirstlane(a); b = __builtin_amdgcn_readfirstlane(b);
          la = a; lb = b;
          if (a > 1000u && a < esty + 250u) esty = a > esty ? min(a, esty + 30u) : esty - ((esty - a) >> 3);
          if (b > 500u && b < estr + 250u) estr = b > estr ? min(b, estr + 30u) : estr - ((estr - b) >> 3);
          const float tpc = (float)(rclk() - tb) * __builtin_amdgcn_rcpf((float)(cclk() - cb));
          c1 = __builtin_amdgcn_readfirstlane((unsigned)((float)esty * tpc)) + PACE_C1;
          c3 = min(__builtin_amdgcn_readfirstlane((unsigned)((float)estr * tpc)) + PACE_C3, 250u);
        }
      }
    }
    else s1 = idx - 125;
  } else {
    if (idx < 72) {
      role_wqk(p, ws, rw, idx, lane_id(), w);
      if (idx < 64) {
        const int last = idx >= 32, q = idx - 32 * last, a0 = S3_SPLIT + last, a1 = NSEG;
        const unsigned f3 = C_HFLAG + 20 * last, f2 = f3 + 12;
        if (q < 12) role_mix<3, 1, 5, false>(p, ws, rw, q, a0, a1, f3, f2, lane_id(), w);
        else if (q < 17) role_mix<2, 1, 15, false>(p, ws, rw, q - 12, a0, a1, f2, C_S1FLAG, lane_id(), w);
        else if (q >= 27) role_mix<1, 1, 20, false>(p, ws, rw, q - 27, a0, a1, C_S1FLAG + 5 + 5 * last, C_TFLAG, lane_id(), w);
        else { s4 = q - 17; s40 = a0; s41 = a1; s4f = f3; s4n = 12; }
      }
    }
    else if (idx < 104) role_v2(p, ws, rw, idx - 72, lane_id(), w);
    else if (idx < 114) s4 = idx - 104;
    else if (idx < 120) role_mix<3, 2, 5, true>(p, ws, rw, idx - 114, 0, S3_SPLIT, C_S3FLAG, C_S2FLAG, lane_id(), w);
    else if (idx < 125) role_mix<2, 1, 15, true>(p, ws, rw, idx - 120, 0, S3_SPLIT, C_S2FLAG, C_S1FLAG, lane_id(), w);
    else if (idx < 127) s1 = idx - 122;
  }
  if (s1 >= 0) role_mix<1, 1, 20, true>(p, ws, rw, s1, 0, S3_SPLIT, C_S1FLAG, C_TFLAG, lane_id(), w);
  if (s4 >= 0) role_mix4(p, ws, rw, s4, s40, s41, s4f, s4n, lane_id(), w);
}

__global__ void k_end() {}

extern "C" void kernel_launch(void* const* d_in, const int* in_sizes, int n_in, void* d_out, int out_size, void* d_ws, size_t ws_size,
                              hipStream_t stream) {
  Params p;
  p.x = (const unsigned short*)d_in[0]; p.mem0 = (const unsigned short*)d_in[1];
  p.wq = (const unsigned short*)d_in[2]; p.wk = (const unsigned short*)d_in[3]; p.wv = (const unsigned short*)d_in[4]; p.wo = (const unsigned short*)d_in[5];
  p.w1 = (const unsigned short*)d_in[8]; p.w2 = (const unsigned short*)d_in[9]; p.w3 = (const unsigned short*)d_in[10]; p.w4 = (const unsigned short*)d_in[11];
  p.out = (unsigned short*)d_out; p.ws = (unsigned char*)d_ws;
  (void)hipFuncSetAttribute((const void*)k_main, hipFuncAttributeMaxDynamicSharedMemorySize, LDS_BYTES);
  k_init<<<INIT_CAL + INIT_CODE + INIT_FILL, 256, 0, stream>>>(p);
  k_main<<<256, 256, LDS_BYTES, stream>>>(p);
}
```
